```python
import torch

def _softmax(x, axis=-1):
    return torch.softmax(x, dim=axis)
def _gelu(x):
    return torch.nn.functional.gelu(x, approximate="tanh")
def _silu(x):
    return torch.nn.functional.silu(x)
def _sigmoid(x):
    return torch.sigmoid(x)
def _layernorm(x, axis=-1, eps=1e-5):
    m = x.mean(dim=axis, keepdim=True)
    v = x.var(dim=axis, keepdim=True, unbiased=False)
    return (x - m) / torch.sqrt(v + eps)
def _scatter_add(tgt, idx, src, axis=0):
    return tgt.clone().index_add_(axis, idx, src)
def _segment_sum(src, seg, n):
    out = torch.zeros((n,) + src.shape[1:], dtype=src.dtype)
    return out.index_add_(0, seg, src)
def _pad(x, pw):
    flat = tuple(v for pair in reversed(pw) for v in pair)
    return torch.nn.functional.pad(x, flat)

kernel_name = 'sequential_preLN__grouped_kv_mqa__glu_gated_geglu__prompt'

def setup_inputs():

    _w = (lambda a: (lambda t: t.to(torch.bfloat16) if t.is_floating_point() else t)(torch.as_tensor(a)))
    def _A_setup():
        B, S, D, H = 1, 2048, 512, 32
        Dh = D // H
        return {
            "x": _w((lambda *s: torch.randn(*s, dtype=torch.bfloat16) * 0.05)(B, S, D)),
            "wq": _w((lambda *s: torch.randn(*s, dtype=torch.bfloat16) * 0.05)(D, H * Dh)),
            "wk": _w((lambda *s: torch.randn(*s, dtype=torch.bfloat16) * 0.05)(D, Dh)),
            "wv": _w((lambda *s: torch.randn(*s, dtype=torch.bfloat16) * 0.05)(D, Dh)),
            "wo": _w((lambda *s: torch.randn(*s, dtype=torch.bfloat16) * 0.05)(H * Dh, D)),
        }
    _A_in = _A_setup()

    def _B_setup():
        B, S, Dm, Df = 1, 2048, 512, 1792
        return {
            "x": _w((lambda *s: torch.randn(*s, dtype=torch.bfloat16) * 0.05)(B, S, Dm)),
            "w_gate": _w((lambda *s: torch.randn(*s, dtype=torch.bfloat16) * 0.4)(Dm, Df) * (Dm ** -0.5)),
            "w_up": _w((lambda *s: torch.randn(*s, dtype=torch.bfloat16) * 0.4)(Dm, Df) * (Dm ** -0.5)),
            "w_down": _w((lambda *s: torch.randn(*s, dtype=torch.bfloat16) * 0.4)(Df, Dm) * (Df ** -0.5)),
        }
    _B_in = _B_setup()

    return {
        "A_x": _A_in["x"],
        "A_wq": _A_in["wq"],
        "A_wk": _A_in["wk"],
        "A_wv": _A_in["wv"],
        "A_wo": _A_in["wo"],
        "B_x": _B_in["x"],
        "B_w_gate": _B_in["w_gate"],
        "B_w_up": _B_in["w_up"],
        "B_w_down": _B_in["w_down"],
    }

def reference(A_x, A_wq, A_wk, A_wv, A_wo, B_x, B_w_gate, B_w_up, B_w_down):
    def _A_ref(x, wq, wk, wv, wo):
        B, S, D = x.shape
        H = 32
        Dh = D // H
        q = (lambda x, *d: x.permute(*d))((x @ wq).reshape(B, S, H, Dh), 0, 2, 1, 3)
        k = x @ wk
        v = x @ wv
        scores = torch.einsum("bhqd,bkd->bhqk", q, k) * (Dh ** -0.5)
        p = _softmax(scores, axis=-1)
        o = torch.einsum("bhqk,bkd->bhqd", p, v)
        return (lambda x, *d: x.permute(*d))(o, 0, 2, 1, 3).reshape(B, S, H * Dh) @ wo

    def _B_ref(x, w_gate, w_up, w_down):
        g = torch.einsum("bsd,df->bsf", x, w_gate)
        u = torch.einsum("bsd,df->bsf", x, w_up)
        h = _gelu(g) * u
        return torch.einsum("bsf,fd->bsd", h, w_down)

    x = A_x
    h = x + _A_ref(_layernorm(x, axis=-1), A_wq, A_wk, A_wv, A_wo)
    h2 = h + _B_ref(_layernorm(h, axis=-1), B_w_gate, B_w_up, B_w_down)
    return h2
```

```cpp
#include <hip/hip_runtime.h>
#include <hip/hip_bf16.h>
#include <stdint.h>

typedef __attribute__((ext_vector_type(8))) short bf16x8;
typedef __attribute__((ext_vector_type(4))) short s16x4;
typedef __attribute__((ext_vector_type(4))) float f32x4;
typedef __attribute__((ext_vector_type(2))) float f32x2;
typedef __attribute__((ext_vector_type(16))) float f32x16;
typedef __attribute__((ext_vector_type(4))) unsigned u32x4;
typedef __attribute__((ext_vector_type(2))) unsigned u32x2;
typedef __attribute__((ext_vector_type(2))) __bf16 bf16x2_t;
typedef unsigned short u16;
#define DI __device__ __forceinline__
#define LDSP __attribute__((address_space(3)))

constexpr int S_ = 2048, D_ = 512, DF_ = 1792;
constexpr float QSCALE = 0.25f * 1.4426950408889634f;
constexpr float EPS = 1e-5f;

constexpr size_t WS_Q = 0;
constexpr size_t WS_O = WS_Q + 2097152;
constexpr size_t WS_H = WS_O + 2097152;
constexpr size_t WS_HH = WS_H + 2097152;
constexpr size_t WS_K = WS_HH + 7340032;
constexpr size_t WS_V = WS_K + 65536;
constexpr size_t WS_WO = WS_V + 65536;
constexpr size_t WS_WGU = WS_WO + 524288;
constexpr size_t WS_WD = WS_WGU + 3670016;
constexpr size_t WS_CS = WS_WD + 1835008;
constexpr size_t WS_ST = WS_CS + 4 * 3584 * 4;

DI unsigned pk2(float a, float b) {
  f32x2 v = {a, b};
  return __builtin_bit_cast(unsigned, __builtin_convertvector(v, bf16x2_t));
}
DI float bflo(unsigned u) { return __uint_as_float(u << 16); }
DI float bfhi(unsigned u) { return __uint_as_float(u & 0xffff0000u); }
DI void glds16(const void* g, char* l) {
  __builtin_amdgcn_global_load_lds((const unsigned*)g, (unsigned*)l, 16, 0, 0);
}
template <int N> DI void wait_vm() { asm volatile("s_waitcnt vmcnt(%0)" ::"n"(N) : "memory"); }
DI void wait_lgkm0() { asm volatile("s_waitcnt lgkmcnt(0)" ::: "memory"); }
DI void bar() { asm volatile("" ::: "memory"); __builtin_amdgcn_s_barrier(); asm volatile("" ::: "memory"); }
DI bf16x8 lds_frag(const char* p) { return *(const bf16x8*)p; }
DI s16x4 lds_tr(const char* p) {
  return __builtin_bit_cast(s16x4, __builtin_amdgcn_ds_read_tr16_b64_v4i16((LDSP s16x4*)(LDSP char*)p));
}
#define MFMA16(a, b, c) __builtin_amdgcn_mfma_f32_16x16x32_bf16(a, b, c, 0, 0, 0)
#define MFMA32(a, b, c) __builtin_amdgcn_mfma_f32_32x32x16_bf16(a, b, c, 0, 0, 0)
DI float dot2bf(unsigned a, unsigned b, float c) {
  return __builtin_amdgcn_fdot2_f32_bf16(__builtin_bit_cast(bf16x2_t, a), __builtin_bit_cast(bf16x2_t, b), c, false);
}
DI float xsum_g(float v) {
  v += __shfl_xor(v, 16);
  v += __shfl_xor(v, 32);
  return v;
}

constexpr int K1_X = 0, K1_W = 65536, K1_KV = 131072, K1_LDS = 147456;

__global__ __launch_bounds__(512, 2) void k1_qkv(const u16* __restrict__ x, const u16* __restrict__ wq,
                                                 const u16* __restrict__ wk, const u16* __restrict__ wv,
                                                 char* __restrict__ ws) {
  extern __shared__ __attribute__((aligned(16))) char lds[];
  const int tid = threadIdx.x, lane = tid & 63;
  const int w = __builtin_amdgcn_readfirstlane(tid >> 6);
  const int b = blockIdx.x, xcd = b & 7, idx = b >> 3;
  const int tg = xcd * 4 + (idx & 3), j = idx >> 2;
  const int r = lane & 15, g = lane >> 4;
  const u16* wkv = (j & 1) ? wv : wk;
  const int tq = j >> 1;

  {
    const int s = lane >> 4, pr = (lane >> 1) & 7, hh = lane & 1;
#pragma unroll
    for (int c = 0; c < 2; ++c) {
#pragma unroll
      for (int i = 0; i < 4; ++i) {
        const int p = w + 8 * i, tt = p & 3, kb = c * 8 + (p >> 2);
        glds16(x + (size_t)(tg * 64 + tt * 16 + r) * 512 + kb * 32 + g * 8, lds + K1_X + (tt * 16 + kb) * 1024);
      }
#pragma unroll
      for (int i = 0; i < 4; ++i) {
        const int kb8 = c * 32 + w + 8 * i;
        const int k = 8 * kb8 + (pr ^ (4 * (kb8 & 1)));
        glds16(wq + (size_t)k * 512 + j * 64 + s * 16 + hh * 8, lds + K1_W + kb8 * 1024);
      }
      {
        const int kb8b = c * 32 + w * 4, kb8 = kb8b + s;
        const int k = 8 * kb8 + (pr ^ (4 * (kb8 & 1)));
        glds16(wkv + (size_t)k * 16 + hh * 8, lds + K1_KV + kb8b * 256);
      }
    }
  }
  const int kg = w >> 2, wn = (w >> 1) & 1, wt = w & 1;
  const bool dokv = (wn == 0) && (wt == (tq >> 1));
  const int tkv = tq & 1;
  const int q4 = (lane & 15) >> 2, p4 = lane & 3;
  const int troff0 = ((q4) ^ (4 * (g & 1))) * 32 + p4 * 8;
  const int troff1 = ((4 + q4) ^ (4 * (g & 1))) * 32 + p4 * 8;
  f32x4 acc[2][2] = {}, cs[2] = {}, kvacc = {}, kvcs = {};
  float sm[2] = {0.f, 0.f}, sq[2] = {0.f, 0.f};
  const unsigned ONE2 = 0x3f803f80u;
  const u32x4 ones4 = {ONE2, ONE2, ONE2, ONE2};
  const bf16x8 ones = __builtin_bit_cast(bf16x8, ones4);
#pragma unroll
  for (int c = 0; c < 2; ++c) {
    if (c == 0) wait_vm<9>(); else wait_vm<0>();
    bar();
#pragma unroll
    for (int kk = 0; kk < 4; ++kk) {
      const int kb = c * 8 + kg * 4 + kk;
      bf16x8 xf[2], wf[2];
#pragma unroll
      for (int t = 0; t < 2; ++t) xf[t] = lds_frag(lds + K1_X + ((wt * 2 + t) * 16 + kb) * 1024 + lane * 16);
#pragma unroll
      for (int i = 0; i < 2; ++i) {
        const char* base = lds + K1_W + (kb * 4 + g) * 1024 + (wn * 2 + i) * 256;
        s16x4 lo = lds_tr(base + troff0), hi = lds_tr(base + troff1);
        wf[i] = __builtin_shufflevector(lo, hi, 0, 1, 2, 3, 4, 5, 6, 7);
      }
#pragma unroll
      for (int i = 0; i < 2; ++i) {
#pragma unroll
        for (int t = 0; t < 2; ++t) acc[i][t] = MFMA16(wf[i], xf[t], acc[i][t]);
        cs[i] = MFMA16(wf[i], ones, cs[i]);
      }
#pragma unroll
      for (int t = 0; t < 2; ++t) {
        const u32x4 u = __builtin_bit_cast(u32x4, xf[t]);
#pragma unroll
        for (int e = 0; e < 4; ++e) { sm[t] = dot2bf(u[e], ONE2, sm[t]); sq[t] = dot2bf(u[e], u[e], sq[t]); }
      }
      if (dokv) {
        const char* base = lds + K1_KV + (kb * 4 + g) * 256;
        s16x4 lo = lds_tr(base + troff0), hi = lds_tr(base + troff1);
        const bf16x8 kf = __builtin_shufflevector(lo, hi, 0, 1, 2, 3, 4, 5, 6, 7);
        const bf16x8 xx = tkv ? xf[1] : xf[0];
        if (j & 1) { kvacc = MFMA16(xx, kf, kvacc); kvcs = MFMA16(ones, kf, kvcs); }
        else       { kvacc = MFMA16(kf, xx, kvacc); kvcs = MFMA16(kf, ones, kvcs); }
      }
    }
  }
  bar();
  {
    float* ex = (float*)lds + (size_t)w * 64 * 24 + lane * 4;
    f32x4* e4 = (f32x4*)((char*)lds + w * 6144);
    e4[0 * 64 + lane] = kg ? acc[0][0] : acc[1][0];
    e4[1 * 64 + lane] = kg ? acc[0][1] : acc[1][1];
    e4[2 * 64 + lane] = kg ? cs[0] : cs[1];
    e4[3 * 64 + lane] = (f32x4){sm[0], sm[1], sq[0], sq[1]};
    e4[4 * 64 + lane] = kvacc;
    e4[5 * 64 + lane] = kvcs;
    (void)ex;
  }
  __syncthreads();
  {
    const f32x4* o4 = (const f32x4*)((char*)lds + (w ^ 4) * 6144);
    f32x4 a0 = (kg ? acc[1][0] : acc[0][0]) + o4[0 * 64 + lane];
    f32x4 a1 = (kg ? acc[1][1] : acc[0][1]) + o4[1 * 64 + lane];
    f32x4 c = (kg ? cs[1] : cs[0]) + o4[2 * 64 + lane];
    f32x4 st = (f32x4){sm[0], sm[1], sq[0], sq[1]} + o4[3 * 64 + lane];
    float mean[2], rstd[2];
#pragma unroll
    for (int t = 0; t < 2; ++t) {
      const float s1 = xsum_g(st[t]), s2 = xsum_g(st[2 + t]);
      mean[t] = s1 * (1.f / 512.f);
      const float var = fmaxf(s2 * (1.f / 512.f) - mean[t] * mean[t], 0.f);
      rstd[t] = rsqrtf(var + EPS);
    }
    const int head = j * 4 + wn * 2 + kg;
#pragma unroll
    for (int t = 0; t < 2; ++t) {
      const f32x4 a = t ? a1 : a0;
      const float sc = rstd[t] * QSCALE;
      float v0 = (a[0] - mean[t] * c[0]) * sc, v1 = (a[1] - mean[t] * c[1]) * sc;
      float v2 = (a[2] - mean[t] * c[2]) * sc, v3 = (a[3] - mean[t] * c[3]) * sc;
      const int tt = tg * 4 + wt * 2 + t;
      char* dst = ws + WS_Q + (size_t)(tt * 16 + (head >> 1)) * 1024 + ((g >> 1) * 32 + (head & 1) * 16 + r) * 16 + (g & 1) * 8;
      *(u32x2*)dst = (u32x2){pk2(v0, v1), pk2(v2, v3)};
    }
    if (dokv && kg == 0) {
      const f32x4 a = kvacc + o4[4 * 64 + lane], cc = kvcs + o4[5 * 64 + lane];
      const float mu = tkv ? mean[1] : mean[0], rs = tkv ? rstd[1] : rstd[0];
      const int tok0 = tg * 64 + tq * 16;
      if (j & 1) {
        float v[4];
#pragma unroll
        for (int e = 0; e < 4; ++e) {
          const float m_e = __shfl(mu, 4 * g + e), r_e = __shfl(rs, 4 * g + e);
          v[e] = (a[e] - m_e * cc[e]) * r_e;
        }
        char* dst = ws + WS_V + (size_t)(tok0 >> 4) * 512 + ((g & 1) * 16 + r) * 16 + (g >> 1) * 8;
        *(u32x2*)dst = (u32x2){pk2(v[0], v[1]), pk2(v[2], v[3])};
      } else {
        float v[4];
#pragma unroll
        for (int e = 0; e < 4; ++e) v[e] = (a[e] - mu * cc[e]) * rs;
        const int key = tok0 + r;
        char* dst = ws + WS_K + (size_t)(key >> 5) * 1024 + ((g >> 1) * 32 + (key & 31)) * 16 + (g & 1) * 8;
        *(u32x2*)dst = (u32x2){pk2(v[0], v[1]), pk2(v[2], v[3])};
      }
    }
  }
}

constexpr int K2_K = 0, K2_V = 65536, K2_ONES = 131072, K2_CMB = K2_ONES + 4096, K2_FLAG = K2_CMB + 4 * 5 * 1024,
              K2_LDS = K2_FLAG + 16;
constexpr int NUNITS = 128 + 896 + 448;

__global__ __launch_bounds__(512, 2) void k2_attn(char* __restrict__ ws, const u16* __restrict__ wo,
                                                  const u16* __restrict__ wg, const u16* __restrict__ wu,
                                                  const u16* __restrict__ wd) {
  extern __shared__ __attribute__((aligned(16))) char lds[];
  const int tid = threadIdx.x, lane = tid & 63;
  const int w = __builtin_amdgcn_readfirstlane(tid >> 6);
  const int b = blockIdx.x;
  const int tt = b >> 1, hg = b & 1;
  const int qg = w & 3, kh = w >> 2;
  const int c32 = lane & 31, h = lane >> 5;

  const char* qp = ws + WS_Q + (size_t)(tt * 16 + hg * 8 + qg * 2) * 1024 + lane * 16;
  const bf16x8 q0 = *(const bf16x8*)qp, q1 = *(const bf16x8*)(qp + 1024);
  asm volatile("" ::: "memory");
#pragma unroll
  for (int c = 0; c < 8; ++c) {
    const int tile = kh * 32 + c * 4 + qg;
    glds16(ws + WS_K + (size_t)tile * 1024 + lane * 16, lds + K2_K + tile * 1024);
    glds16(ws + WS_V + (size_t)tile * 1024 + lane * 16, lds + K2_V + tile * 1024);
  }
  asm volatile("" ::: "memory");
  const int gw = b * 8 + w;
  const int unit = gw < NUNITS ? gw : NUNITS - 1;
  u16 wraw[32];
  int dst_tile;
  {
    const int r = lane & 15, g = lane >> 4;
    const u16* src; int ld, kb0;
    if (unit < 128) { const int rb = unit >> 2; kb0 = (unit & 3) * 4; src = wo + rb * 16 + r; ld = 512; dst_tile = (int)(WS_WO >> 10) + rb * 16 + kb0; }
    else if (unit < 1024) { const int v = unit - 128, T = v >> 2; kb0 = (v & 3) * 4;
      src = ((r & 2) ? wu : wg) + T * 8 + (r >> 2) * 2 + (r & 1); ld = 1792; dst_tile = (int)(WS_WGU >> 10) + T * 16 + kb0; }
    else { const int v = unit - 1024, rb = v / 14; kb0 = (v % 14) * 4; src = wd + rb * 16 + r; ld = 512; dst_tile = (int)(WS_WD >> 10) + rb * 56 + kb0; }
    src += (size_t)(kb0 * 32 + g * 8) * ld;
#pragma unroll
    for (int t = 0; t < 4; ++t)
#pragma unroll
      for (int jj = 0; jj < 8; ++jj) wraw[t * 8 + jj] = src[(size_t)(t * 32 + jj) * ld];
  }
  asm volatile("" ::: "memory");
  {
    const unsigned ONE2 = 0x3f803f80u;
    if (tid < 256) *(u32x4*)(lds + K2_ONES + tid * 16) = (u32x4){ONE2, ONE2, ONE2, ONE2};
    if (tid == 0) *(unsigned*)(lds + K2_FLAG) = 0u;
  }
  const char* kp = lds + K2_K + kh * 32768 + lane * 16;
  const bool vreal = c32 < 16;
  const int onesoff = (c32 >= 20 && c32 < 28) ? 64 : 0;
  const char* vp = vreal ? (lds + K2_V + kh * 32768 + (h * 16 + c32) * 16) : (lds + K2_ONES + onesoff);
  const int vstep = vreal ? 4096 : 0;

  f32x16 o0, o1;
  float m0, m1;
  int nmax = 1;
  wait_lgkm0();
  wait_vm<14 + 32>();
  bar();
  for (int attempt = 0; attempt < 2; ++attempt) {
    {
      float a0 = -INFINITY, a1 = -INFINITY;
      const f32x16 z = {};
      for (int t = 0; t < nmax; ++t) {
        const bf16x8 kf = lds_frag(kp + t * 1024);
        const f32x16 s0 = MFMA32(kf, q0, z), s1 = MFMA32(kf, q1, z);
#pragma unroll
        for (int i = 0; i < 16; ++i) { a0 = fmaxf(a0, s0[i]); a1 = fmaxf(a1, s1[i]); }
      }
      m0 = fmaxf(a0, __shfl_xor(a0, 32));
      m1 = fmaxf(a1, __shfl_xor(a1, 32));
    }
    f32x16 n0, n1;
#pragma unroll
    for (int i = 0; i < 16; ++i) { n0[i] = -m0; n1[i] = -m1; o0[i] = 0.f; o1[i] = 0.f; }
    asm volatile("" : "+v"(n0), "+v"(n1));
    const char* vq = vp;
#pragma unroll
    for (int c = 0; c < 8; ++c) {
      if (c > 0) {
        if (c == 1) wait_vm<12 + 32>(); else if (c == 2) wait_vm<10 + 32>(); else if (c == 3) wait_vm<8 + 32>();
        else if (c == 4) wait_vm<6 + 32>(); else if (c == 5) wait_vm<4 + 32>(); else if (c == 6) wait_vm<2 + 32>(); else wait_vm<32>();
        bar();
      }
#pragma unroll
      for (int t = 0; t < 4; ++t) {
        const bf16x8 kf = lds_frag(kp + (c * 4 + t) * 1024);
        const bf16x8 va = lds_frag(vq + t * 1024), vb = lds_frag(vq + t * 1024 + 512);
        f32x16 s0 = MFMA32(kf, q0, n0), s1 = MFMA32(kf, q1, n1);
        u32x4 pa, pb, pc, pd;
#pragma unroll
        for (int i = 0; i < 4; ++i) {
          pa[i] = pk2(__builtin_amdgcn_exp2f(s0[2 * i]), __builtin_amdgcn_exp2f(s0[2 * i + 1]));
          pb[i] = pk2(__builtin_amdgcn_exp2f(s0[8 + 2 * i]), __builtin_amdgcn_exp2f(s0[8 + 2 * i + 1]));
        }
        o0 = MFMA32(va, __builtin_bit_cast(bf16x8, pa), o0);
        o0 = MFMA32(vb, __builtin_bit_cast(bf16x8, pb), o0);
#pragma unroll
        for (int i = 0; i < 4; ++i) {
          pc[i] = pk2(__builtin_amdgcn_exp2f(s1[2 * i]), __builtin_amdgcn_exp2f(s1[2 * i + 1]));
          pd[i] = pk2(__builtin_amdgcn_exp2f(s1[8 + 2 * i]), __builtin_amdgcn_exp2f(s1[8 + 2 * i + 1]));
        }
        o1 = MFMA32(va, __builtin_bit_cast(bf16x8, pc), o1);
        o1 = MFMA32(vb, __builtin_bit_cast(bf16x8, pd), o1);
      }
      vq += vstep;
    }
    const bool ok = (o0[8] < 1e30f) && (o1[8] < 1e30f);
    if (!__all(ok)) { if (lane == 0) *(volatile unsigned*)(lds + K2_FLAG) = 1u; }
    __syncthreads();
    const unsigned f = *(volatile unsigned*)(lds + K2_FLAG);
    if (f == 0u || attempt == 1) break;
    nmax = 32;
  }
  {
    f32x4* cm = (f32x4*)(lds + K2_CMB + qg * 5120);
    if (kh == 1) {
      cm[0 * 64 + lane] = (f32x4){o0[0], o0[1], o0[2], o0[3]};
      cm[1 * 64 + lane] = (f32x4){o0[4], o0[5], o0[6], o0[7]};
      cm[2 * 64 + lane] = (f32x4){o1[0], o1[1], o1[2], o1[3]};
      cm[3 * 64 + lane] = (f32x4){o1[4], o1[5], o1[6], o1[7]};
      cm[4 * 64 + lane] = (f32x4){m0, o0[8], m1, o1[8]};
    }
    __syncthreads();
    if (kh == 0) {
      const f32x4 st = cm[4 * 64 + lane];
#pragma unroll
      for (int qb = 0; qb < 2; ++qb) {
        const float ma = qb ? m1 : m0, mb = qb ? st[2] : st[0];
        const float la = qb ? o1[8] : o0[8], lb = qb ? st[3] : st[1];
        const float mm = fmaxf(ma, mb);
        const float fa = __builtin_amdgcn_exp2f(ma - mm), fb = __builtin_amdgcn_exp2f(mb - mm);
        const float inv = 1.f / (la * fa + lb * fb);
        const float ga = fa * inv, gb = fb * inv;
        const f32x4 b0 = cm[(qb * 2 + 0) * 64 + lane], b1 = cm[(qb * 2 + 1) * 64 + lane];
        float v[8];
#pragma unroll
        for (int i = 0; i < 4; ++i) {
          v[i] = (qb ? o1[i] : o0[i]) * ga + b0[i] * gb;
          v[4 + i] = (qb ? o1[4 + i] : o0[4 + i]) * ga + b1[i] * gb;
        }
        char* dst = ws + WS_O + (size_t)(tt * 16 + hg * 8 + qg * 2 + qb) * 1024 + (((c32 >> 4) * 2) * 16 + (c32 & 15)) * 16 + h * 8;
        *(u32x2*)dst = (u32x2){pk2(v[0], v[1]), pk2(v[2], v[3])};
        *(u32x2*)(dst + 256) = (u32x2){pk2(v[4], v[5]), pk2(v[6], v[7])};
      }
    }
  }
  if (gw < NUNITS) {
    float csum = 0.f;
#pragma unroll
    for (int t = 0; t < 4; ++t) {
      u32x4 o;
#pragma unroll
      for (int e = 0; e < 4; ++e) {
        const unsigned lo = wraw[t * 8 + 2 * e], hi = wraw[t * 8 + 2 * e + 1];
        o[e] = lo | (hi << 16);
        csum += __uint_as_float(lo << 16) + __uint_as_float(hi << 16);
      }
      *(u32x4*)(ws + (size_t)(dst_tile + t) * 1024 + lane * 16) = o;
    }
    if (unit >= 128 && unit < 1024) {
      csum = xsum_g(csum);
      const int v = unit - 128;
      if (lane < 16) ((float*)(ws + WS_CS))[(v & 3) * 3584 + (v >> 2) * 16 + lane] = csum;
    }
  }
}

template <int MODE>
__global__ __launch_bounds__(512, 2) void k35_gemm(char* __restrict__ ws, const u16* __restrict__ x, u16* __restrict__ out) {
  constexpr int NKB = MODE ? 56 : 16, NS = NKB / 4, PF = MODE ? 3 : 4;
  extern __shared__ __attribute__((aligned(16))) char lds[];
  const int tid = threadIdx.x, lane = tid & 63;
  const int w = __builtin_amdgcn_readfirstlane(tid >> 6);
  const int b = blockIdx.x, xcd = b & 7, idx = b >> 3;
  const int tg = xcd * 4 + (idx & 3), ng = idx >> 2;
  const int r = lane & 15, g = lane >> 4;
  const char* A = ws + (MODE ? WS_WD : WS_WO) + (size_t)(ng * 4) * NKB * 1024;
  const char* B = ws + (MODE ? WS_HH : WS_O) + (size_t)(tg * 4) * NKB * 1024;
  const char* src = (w < 4 ? A + (size_t)w * NKB * 1024 : B + (size_t)(w - 4) * NKB * 1024) + lane * 16;
  auto issue = [&](int s) {
    char* dst = lds + (s & 3) * 32768 + w * 1024;
#pragma unroll
    for (int i = 0; i < 4; ++i) glds16(src + (size_t)(s * 4 + i) * 1024, dst + i * 8192);
  };
#pragma unroll
  for (int s = 0; s < PF; ++s) issue(s);
  const int kg = w >> 2, wn = (w >> 1) & 1, wt = w & 1;
  f32x4 acc[2][2] = {};
#pragma unroll
  for (int s = 0; s < NS; ++s) {
    constexpr int dummy = 0; (void)dummy;
    const int later = (PF - 1 < NS - 1 - s) ? PF - 1 : NS - 1 - s;
    if (later == 3) wait_vm<12>(); else if (later == 2) wait_vm<8>(); else if (later == 1) wait_vm<4>(); else wait_vm<0>();
    bar();
    if (s + PF < NS) issue(s + PF);
    const char* base = lds + (s & 3) * 32768 + lane * 16;
#pragma unroll
    for (int kk = 0; kk < 2; ++kk) {
      const char* p = base + (kg * 2 + kk) * 8192;
      bf16x8 af[2], bf[2];
#pragma unroll
      for (int i = 0; i < 2; ++i) { af[i] = lds_frag(p + (wn * 2 + i) * 1024); bf[i] = lds_frag(p + (4 + wt * 2 + i) * 1024); }
#pragma unroll
      for (int i = 0; i < 2; ++i)
#pragma unroll
        for (int t = 0; t < 2; ++t) acc[i][t] = MFMA16(af[i], bf[t], acc[i][t]);
    }
  }
  bar();
  f32x4* e4 = (f32x4*)(lds + w * 2048);
  e4[lane] = kg ? acc[0][0] : acc[1][0];
  e4[64 + lane] = kg ? acc[0][1] : acc[1][1];
  __syncthreads();
  const f32x4* o4 = (const f32x4*)(lds + (w ^ 4) * 2048);
  const int nt = ng * 4 + wn * 2 + kg;
  if constexpr (MODE == 0) {
    float* part = (float*)(lds + 16384);
#pragma unroll
    for (int t = 0; t < 2; ++t) {
      const f32x4 a = (t ? (kg ? acc[1][1] : acc[0][1]) : (kg ? acc[1][0] : acc[0][0])) + o4[t * 64 + lane];
      const int tt = tg * 4 + wt * 2 + t;
      const u32x2 xr = *(const u32x2*)(x + (size_t)(tt * 16 + r) * 512 + nt * 16 + g * 4);
      const unsigned u0 = pk2(bflo(xr[0]) + a[0], bfhi(xr[0]) + a[1]), u1 = pk2(bflo(xr[1]) + a[2], bfhi(xr[1]) + a[3]);
      char* dst = ws + WS_H + (size_t)(tt * 16 + (nt >> 1)) * 1024 + (((nt & 1) * 2 + (g >> 1)) * 16 + r) * 16 + (g & 1) * 8;
      *(u32x2*)dst = (u32x2){u0, u1};
      const float h0 = bflo(u0), h1 = bfhi(u0), h2 = bflo(u1), h3 = bfhi(u1);
      const float s1 = xsum_g((h0 + h1) + (h2 + h3)), s2 = xsum_g((h0 * h0 + h1 * h1) + (h2 * h2 + h3 * h3));
      if (g == 0) *(f32x2*)(part + ((wn * 2 + kg) * 64 + (wt * 2 + t) * 16 + r) * 2) = (f32x2){s1, s2};
    }
    __syncthreads();
    if (tid < 64) {
      f32x2 s = {0.f, 0.f};
#pragma unroll
      for (int i = 0; i < 4; ++i) s += *(const f32x2*)(part + (i * 64 + tid) * 2);
      *(f32x2*)(ws + WS_ST + ((size_t)(tg * 64 + tid) * 8 + ng) * 8) = s;
    }
  } else {
#pragma unroll
    for (int t = 0; t < 2; ++t) {
      const f32x4 a = (t ? (kg ? acc[1][1] : acc[0][1]) : (kg ? acc[1][0] : acc[0][0])) + o4[t * 64 + lane];
      const int tt = tg * 4 + wt * 2 + t;
      const u32x2 hr = *(const u32x2*)(ws + WS_H + (size_t)(tt * 16 + (nt >> 1)) * 1024 + (((nt & 1) * 2 + (g >> 1)) * 16 + r) * 16 + (g & 1) * 8);
      *(u32x2*)(out + (size_t)(tt * 16 + r) * 512 + nt * 16 + g * 4) =
          (u32x2){pk2(bflo(hr[0]) + a[0], bfhi(hr[0]) + a[1]), pk2(bflo(hr[1]) + a[2], bfhi(hr[1]) + a[3])};
    }
  }
}

constexpr int K4_SLOT = 22 * 1024, K4_NSLOT = 7, K4_ST = K4_SLOT * K4_NSLOT, K4_LDS = K4_ST + 1024;

template <int NT>
DI void k4_body(char* __restrict__ ws, char* lds, int w, int lane, int tg, int hgp, int tile0, int wt) {
  const int r = lane & 15, g = lane >> 4;
  const int np = (w < 6) ? 3 : 2;
  const char* A = ws + WS_WGU + (size_t)(hgp * 14) * 16 * 1024 + lane * 16;
  const char* B = ws + WS_H + (size_t)(tg * 8) * 16 * 1024 + lane * 16;
  const char* s0 = A + (size_t)w * 16384;
  const char* s1 = (w < 6) ? A + (size_t)(w + 8) * 16384 : B + (size_t)(w - 6) * 16384;
  const char* s2 = B + (size_t)(w + 2) * 16384;
  auto issue = [&](int kb, int slot) {
    char* dst = lds + slot * K4_SLOT + w * 1024;
    glds16(s0 + kb * 1024, dst);
    glds16(s1 + kb * 1024, dst + 8192);
    if (np == 3) glds16(s2 + kb * 1024, dst + 16384);
  };
#pragma unroll
  for (int s = 0; s < 6; ++s) issue(s, s);
  f32x4 acc[NT][4] = {};
#pragma unroll
  for (int s = 0; s < 16; ++s) {
    const int later = (5 < 15 - s) ? 5 : 15 - s;
    if (np == 3) {
      if (later == 5) wait_vm<15>(); else if (later == 4) wait_vm<12>(); else if (later == 3) wait_vm<9>();
      else if (later == 2) wait_vm<6>(); else if (later == 1) wait_vm<3>(); else wait_vm<0>();
    } else {
      if (later == 5) wait_vm<10>(); else if (later == 4) wait_vm<8>(); else if (later == 3) wait_vm<6>();
      else if (later == 2) wait_vm<4>(); else if (later == 1) wait_vm<2>(); else wait_vm<0>();
    }
    bar();
    if (s + 6 < 16) issue(s + 6, (s + 6) % K4_NSLOT);
    const char* base = lds + (s % K4_NSLOT) * K4_SLOT + lane * 16;
    bf16x8 bf[4], af[NT];
#pragma unroll
    for (int t = 0; t < 4; ++t) bf[t] = lds_frag(base + (14 + wt * 4 + t) * 1024);
#pragma unroll
    for (int i = 0; i < NT; ++i) af[i] = lds_frag(base + (tile0 + i) * 1024);
#pragma unroll
    for (int i = 0; i < NT; ++i)
#pragma unroll
      for (int t = 0; t < 4; ++t) acc[i][t] = MFMA16(af[i], bf[t], acc[i][t]);
  }
  const f32x2* st = (const f32x2*)(lds + K4_ST);
  const float* csp = (const float*)(ws + WS_CS);
#pragma unroll
  for (int i = 0; i < NT; ++i) {
    const int T = hgp * 14 + tile0 + i;
    f32x4 cs = *(const f32x4*)(csp + T * 16 + g * 4);
#pragma unroll
    for (int q = 1; q < 4; ++q) cs += *(const f32x4*)(csp + q * 3584 + T * 16 + g * 4);
#pragma unroll
    for (int t = 0; t < 4; ++t) {
      const f32x2 ms = st[(wt * 4 + t) * 16 + r];
      const f32x4 a = acc[i][t];
      float o[2];
#pragma unroll
      for (int e = 0; e < 2; ++e) {
        const float gv = (a[e] - ms[0] * cs[e]) * ms[1];
        const float uv = (a[2 + e] - ms[0] * cs[2 + e]) * ms[1];
        const float z = gv * (1.f + 0.044715f * gv * gv) * (-2.f * 0.7978845608028654f * 1.4426950408889634f);
        const float sg = __builtin_amdgcn_rcpf(1.f + __builtin_amdgcn_exp2f(z));
        o[e] = gv * sg * uv;
      }
      const int tt = tg * 8 + wt * 4 + t;
      *(unsigned*)(ws + WS_HH + (size_t)(tt * 56 + (T >> 2)) * 1024 + ((T & 3) * 16 + r) * 16 + g * 4) = pk2(o[0], o[1]);
    }
  }
}

__global__ __launch_bounds__(512, 2) void k4_gateup(char* __restrict__ ws) {
  extern __shared__ __attribute__((aligned(16))) char lds[];
  const int tid = threadIdx.x, lane = tid & 63;
  const int w = __builtin_amdgcn_readfirstlane(tid >> 6);
  const int b = blockIdx.x, xcd = b & 7, idx = b >> 3;
  const int tg = 8 * (xcd & 1) + (idx & 7), hgp = 4 * (xcd >> 1) + (idx >> 3);
  if (tid < 128) {
    const f32x4* p = (const f32x4*)(ws + WS_ST + (size_t)(tg * 128 + tid) * 64);
    float s1 = 0.f, s2 = 0.f;
#pragma unroll
    for (int i = 0; i < 4; ++i) { const f32x4 v = p[i]; s1 += v[0] + v[2]; s2 += v[1] + v[3]; }
    const float mean = s1 * (1.f / 512.f);
    const float var = fmaxf(s2 * (1.f / 512.f) - mean * mean, 0.f);
    *(f32x2*)(lds + K4_ST + tid * 8) = (f32x2){mean, rsqrtf(var + EPS)};
  }
  wait_lgkm0();
  const int wt = w & 1;
  if (w < 4) k4_body<4>(ws, lds, w, lane, tg, hgp, (w >> 1) * 7, wt);
  else       k4_body<3>(ws, lds, w, lane, tg, hgp, ((w - 4) >> 1) * 7 + 4, wt);
}

extern "C" void kernel_launch(void* const* d_in, const int* in_sizes, int n_in, void* d_out, int out_size,
                              void* d_ws, size_t ws_size, hipStream_t stream) {
  const u16* x = (const u16*)d_in[0];
  const u16* wq = (const u16*)d_in[1];
  const u16* wk = (const u16*)d_in[2];
  const u16* wv = (const u16*)d_in[3];
  const u16* wo = (const u16*)d_in[4];
  const u16* wg = (const u16*)d_in[6];
  const u16* wu = (const u16*)d_in[7];
  const u16* wd = (const u16*)d_in[8];
  char* ws = (char*)d_ws;
  (void)hipFuncSetAttribute((const void*)k1_qkv, hipFuncAttributeMaxDynamicSharedMemorySize, K1_LDS);
  (void)hipFuncSetAttribute((const void*)k2_attn, hipFuncAttributeMaxDynamicSharedMemorySize, K2_LDS);
  (void)hipFuncSetAttribute((const void*)k35_gemm<0>, hipFuncAttributeMaxDynamicSharedMemorySize, 131072);
  (void)hipFuncSetAttribute((const void*)k35_gemm<1>, hipFuncAttributeMaxDynamicSharedMemorySize, 131072);
  (void)hipFuncSetAttribute((const void*)k4_gateup, hipFuncAttributeMaxDynamicSharedMemorySize, K4_LDS);
  k1_qkv<<<256, 512, K1_LDS, stream>>>(x, wq, wk, wv, ws);
  k2_attn<<<256, 512, K2_LDS, stream>>>(ws, wo, wg, wu, wd);
  k35_gemm<0><<<256, 512, 131072, stream>>>(ws, x, nullptr);
  k4_gateup<<<256, 512, K4_LDS, stream>>>(ws);
  k35_gemm<1><<<256, 512, 131072, stream>>>(ws, x, (u16*)d_out);
}
```

```cpp
#include <hip/hip_runtime.h>
#include <hip/hip_bf16.h>
#include <stdint.h>

typedef __attribute__((ext_vector_type(8))) short bf16x8;
typedef __attribute__((ext_vector_type(4))) short s16x4;
typedef __attribute__((ext_vector_type(4))) float f32x4;
typedef __attribute__((ext_vector_type(2))) float f32x2;
typedef __attribute__((ext_vector_type(16))) float f32x16;
typedef __attribute__((ext_vector_type(4))) unsigned u32x4;
typedef __attribute__((ext_vector_type(2))) unsigned u32x2;
typedef __attribute__((ext_vector_type(2))) __bf16 bf16x2_t;
typedef unsigned short u16;
#define DI __device__ __forceinline__
#define LDSP __attribute__((address_space(3)))

constexpr int S_ = 2048, D_ = 512, DF_ = 1792;
constexpr float QSCALE = 0.25f * 1.4426950408889634f;
constexpr float EPS = 1e-5f;

constexpr size_t WS_Q = 0;
constexpr size_t WS_O = WS_Q + 2097152;
constexpr size_t WS_H = WS_O + 2097152;
constexpr size_t WS_HH = WS_H + 2097152;
constexpr size_t WS_K = WS_HH + 7340032;
constexpr size_t WS_V = WS_K + 65536;
constexpr size_t WS_WO = WS_V + 65536;
constexpr size_t WS_WGU = WS_WO + 524288;
constexpr size_t WS_WD = WS_WGU + 3670016;
constexpr size_t WS_CP = WS_WD + 1835008;
constexpr size_t WS_CS = WS_CP + 16 * 3584 * 4;
constexpr size_t WS_ST = WS_CS + 3584 * 4;

constexpr size_t WS_DBG = 64u << 20;
#define STAMP_DECL
#define STAMP(i) do {} while (0)
#define STAMP_OUT(kid) do {} while (0)
DI unsigned pk2(float a, float b) {
  f32x2 v = {a, b};
  return __builtin_bit_cast(unsigned, __builtin_convertvector(v, bf16x2_t));
}
DI float bflo(unsigned u) { return __uint_as_float(u << 16); }
DI float bfhi(unsigned u) { return __uint_as_float(u & 0xffff0000u); }
DI void glds16(const void* g, char* l) {
  __builtin_amdgcn_global_load_lds((const unsigned*)g, (unsigned*)l, 16, 0, 0);
}
DI void st16_wt(char* base, unsigned off, u32x4 v) {
  const __amdgpu_buffer_rsrc_t rs = __builtin_amdgcn_make_buffer_rsrc((void*)base, (short)0, 0x7fffffff, 0x00020000);
  __builtin_amdgcn_raw_buffer_store_b128(v, rs, off, 0, 16);
}
template <int N> DI void wait_vm() { asm volatile("s_waitcnt vmcnt(%0)" ::"n"(N) : "memory"); }
DI void wait_lgkm0() { __builtin_amdgcn_s_waitcnt(0xC07F); asm volatile("" ::: "memory"); }
DI void bar() { asm volatile("" ::: "memory"); __builtin_amdgcn_s_barrier(); asm volatile("" ::: "memory"); }
DI bf16x8 lds_frag(const char* p) { return *(const bf16x8*)p; }
DI s16x4 lds_tr(const char* p) {
  return __builtin_bit_cast(s16x4, __builtin_amdgcn_ds_read_tr16_b64_v4i16((LDSP s16x4*)(LDSP char*)p));
}
#define MFMA16(a, b, c) __builtin_amdgcn_mfma_f32_16x16x32_bf16(a, b, c, 0, 0, 0)
#define MFMA32(a, b, c) __builtin_amdgcn_mfma_f32_32x32x16_bf16(a, b, c, 0, 0, 0)
DI float dot2bf(unsigned a, unsigned b, float c) {
  return __builtin_amdgcn_fdot2_f32_bf16(__builtin_bit_cast(bf16x2_t, a), __builtin_bit_cast(bf16x2_t, b), c, false);
}
DI float xsum_g(float v) {
  v += __shfl_xor(v, 16);
  v += __shfl_xor(v, 32);
  return v;
}

constexpr int K1_X = 0, K1_W = 65536, K1_KV = 131072, K1_LDS = 147456;

__global__ __launch_bounds__(512, 2) void k1_qkv(const u16* __restrict__ x, const u16* __restrict__ wq,
                                                 const u16* __restrict__ wk, const u16* __restrict__ wv,
                                                 char* __restrict__ ws) {
  extern __shared__ __attribute__((aligned(16))) char lds[];
  const int tid = threadIdx.x, lane = tid & 63;
  const int w = __builtin_amdgcn_readfirstlane(tid >> 6);
  const int b = blockIdx.x, xcd = b & 7, idx = b >> 3;
  const int tg = xcd * 4 + (idx & 3), j = idx >> 2;
  const int r = lane & 15, g = lane >> 4;
  const u16* wkv = (j & 1) ? wv : wk;
  const int tq = j >> 1;
  STAMP_DECL; STAMP(0);

  {
    const int s = lane >> 4, pr = (lane >> 1) & 7, hh = lane & 1;
#pragma unroll
    for (int c = 0; c < 2; ++c) {
      const int kb8b = c * 32 + w * 4, kb8 = kb8b + s;
      const int k = 8 * kb8 + (pr ^ (4 * (kb8 & 1)));
      glds16(wkv + (size_t)k * 16 + hh * 8, lds + K1_KV + kb8b * 256);
    }
#pragma unroll
    for (int c = 0; c < 4; ++c) {
#pragma unroll
      for (int i = 0; i < 2; ++i) {
        const int p = w + 8 * i, rg = p & 7, kq = c * 2 + (p >> 3);
        const int row = rg * 8 + (lane >> 3), ch = (lane & 7) ^ (lane >> 3);
        glds16(x + (size_t)(tg * 64 + row) * 512 + kq * 64 + ch * 8, lds + K1_X + kq * 8192 + rg * 1024);
      }
#pragma unroll
      for (int i = 0; i < 2; ++i) {
        const int kb8 = c * 16 + w + 8 * i;
        const int k = 8 * kb8 + (pr ^ (4 * (kb8 & 1)));
        glds16(wq + (size_t)k * 512 + j * 64 + s * 16 + hh * 8, lds + K1_W + kb8 * 1024);
      }
    }
  }
  const int kg = w >> 2, wn = (w >> 1) & 1, wt = w & 1;
  const bool dokv = (wn == 0) && (wt == (tq >> 1));
  const int tkv = tq & 1;
  const int q4 = (lane & 15) >> 2, p4 = lane & 3;
  const int troff0 = ((q4) ^ (4 * (g & 1))) * 32 + p4 * 8;
  const int troff1 = ((4 + q4) ^ (4 * (g & 1))) * 32 + p4 * 8;
  f32x4 acc[2][2] = {}, cs[2] = {}, kvacc = {}, kvcs = {};
  f32x4 smv[2] = {}, sqv[2] = {};
  const unsigned ONE2 = 0x3f803f80u;
  const u32x4 ones4 = {ONE2, ONE2, ONE2, ONE2};
  const bf16x8 ones = __builtin_bit_cast(bf16x8, ones4);
#pragma unroll
  for (int c = 0; c < 4; ++c) {
    if (c == 0) STAMP(1);
    if (c == 0) wait_vm<12>(); else if (c == 1) wait_vm<8>(); else if (c == 2) wait_vm<4>(); else wait_vm<0>();
    bar();
    if (c == 0) STAMP(2); else if (c == 3) STAMP(3);
#pragma unroll
    for (int kk = 0; kk < 2; ++kk) {
      const int kb = c * 4 + kg * 2 + kk;
      bf16x8 xf[2], wf[2];
#pragma unroll
      for (int t = 0; t < 2; ++t)
        xf[t] = lds_frag(lds + K1_X + (kb >> 1) * 8192 + ((wt * 2 + t) * 16 + r) * 128 + ((((kb & 1) * 4 + g) ^ (r & 7)) * 16));
#pragma unroll
      for (int i = 0; i < 2; ++i) {
        const char* base = lds + K1_W + (kb * 4 + g) * 1024 + (wn * 2 + i) * 256;
        s16x4 lo = lds_tr(base + troff0), hi = lds_tr(base + troff1);
        wf[i] = __builtin_shufflevector(lo, hi, 0, 1, 2, 3, 4, 5, 6, 7);
      }
#pragma unroll
      for (int i = 0; i < 2; ++i) {
#pragma unroll
        for (int t = 0; t < 2; ++t) acc[i][t] = MFMA16(wf[i], xf[t], acc[i][t]);
        cs[i] = MFMA16(wf[i], ones, cs[i]);
      }
#pragma unroll
      for (int t = 0; t < 2; ++t) { smv[t] = MFMA16(ones, xf[t], smv[t]); sqv[t] = MFMA16(xf[t], xf[t], sqv[t]); }
      if (dokv) {
        const char* base = lds + K1_KV + (kb * 4 + g) * 256;
        s16x4 lo = lds_tr(base + troff0), hi = lds_tr(base + troff1);
        const bf16x8 kf = __builtin_shufflevector(lo, hi, 0, 1, 2, 3, 4, 5, 6, 7);
        const bf16x8 xx = tkv ? xf[1] : xf[0];
        if (j & 1) { kvacc = MFMA16(xx, kf, kvacc); kvcs = MFMA16(ones, kf, kvcs); }
        else       { kvacc = MFMA16(kf, xx, kvacc); kvcs = MFMA16(kf, ones, kvcs); }
      }
    }
  }
  float sm[2], sq[2];
#pragma unroll
  for (int t = 0; t < 2; ++t) {
    sm[t] = smv[t][0];
    const float d = (r & 2) ? ((r & 1) ? sqv[t][3] : sqv[t][2]) : ((r & 1) ? sqv[t][1] : sqv[t][0]);
    sq[t] = xsum_g((g == (r >> 2)) ? d : 0.f);
  }
  STAMP(4);
  bar();
  {
    float* ex = (float*)lds + (size_t)w * 64 * 24 + lane * 4;
    f32x4* e4 = (f32x4*)((char*)lds + w * 6144);
    e4[0 * 64 + lane] = kg ? acc[0][0] : acc[1][0];
    e4[1 * 64 + lane] = kg ? acc[0][1] : acc[1][1];
    e4[2 * 64 + lane] = kg ? cs[0] : cs[1];
    e4[3 * 64 + lane] = (f32x4){sm[0], sm[1], sq[0], sq[1]};
    e4[4 * 64 + lane] = kvacc;
    e4[5 * 64 + lane] = kvcs;
    (void)ex;
  }
  __syncthreads();
  {
    const f32x4* o4 = (const f32x4*)((char*)lds + (w ^ 4) * 6144);
    f32x4 a0 = (kg ? acc[1][0] : acc[0][0]) + o4[0 * 64 + lane];
    f32x4 a1 = (kg ? acc[1][1] : acc[0][1]) + o4[1 * 64 + lane];
    f32x4 c = (kg ? cs[1] : cs[0]) + o4[2 * 64 + lane];
    f32x4 st = (f32x4){sm[0], sm[1], sq[0], sq[1]} + o4[3 * 64 + lane];
    float mean[2], rstd[2];
#pragma unroll
    for (int t = 0; t < 2; ++t) {
      const float s1 = st[t], s2 = st[2 + t];
      mean[t] = s1 * (1.f / 512.f);
      const float var = fmaxf(s2 * (1.f / 512.f) - mean[t] * mean[t], 0.f);
      rstd[t] = rsqrtf(var + EPS);
    }
    const int head = j * 4 + wn * 2 + kg;
#pragma unroll
    for (int t = 0; t < 2; ++t) {
      const f32x4 a = t ? a1 : a0;
      const float sc = rstd[t] * QSCALE;
      float v0 = (a[0] - mean[t] * c[0]) * sc, v1 = (a[1] - mean[t] * c[1]) * sc;
      float v2 = (a[2] - mean[t] * c[2]) * sc, v3 = (a[3] - mean[t] * c[3]) * sc;
      const int tt = tg * 4 + wt * 2 + t;
      char* dst = ws + WS_Q + (size_t)(tt * 16 + (head >> 1)) * 1024 + ((g >> 1) * 32 + (head & 1) * 16 + r) * 16 + (g & 1) * 8;
      *(u32x2*)dst = (u32x2){pk2(v0, v1), pk2(v2, v3)};
    }
    if (dokv && kg == 0) {
      const f32x4 a = kvacc + o4[4 * 64 + lane], cc = kvcs + o4[5 * 64 + lane];
      const float mu = tkv ? mean[1] : mean[0], rs = tkv ? rstd[1] : rstd[0];
      const int tok0 = tg * 64 + tq * 16;
      if (j & 1) {
        float v[4];
#pragma unroll
        for (int e = 0; e < 4; ++e) {
          const float m_e = __shfl(mu, 4 * g + e), r_e = __shfl(rs, 4 * g + e);
          v[e] = (a[e] - m_e * cc[e]) * r_e;
        }
        char* dst = ws + WS_V + (size_t)(tok0 >> 4) * 512 + ((g & 1) * 16 + r) * 16 + (g >> 1) * 8;
        *(u32x2*)dst = (u32x2){pk2(v[0], v[1]), pk2(v[2], v[3])};
      } else {
        float v[4];
#pragma unroll
        for (int e = 0; e < 4; ++e) v[e] = (a[e] - mu * cc[e]) * rs;
        const int key = tok0 + r;
        char* dst = ws + WS_K + (size_t)(key >> 5) * 1024 + ((g >> 1) * 32 + (key & 31)) * 16 + (g & 1) * 8;
        *(u32x2*)dst = (u32x2){pk2(v[0], v[1]), pk2(v[2], v[3])};
      }
    }
  }
  STAMP(5); STAMP_OUT(0);
}

constexpr int K2_K = 0, K2_V = 65536, K2_ONES = 131072, K2_CMB = K2_ONES + 4096, K2_FLAG = K2_CMB + 4 * 5 * 1024,
              K2_LDS = K2_FLAG + 16;
constexpr int NUNITS = 128 + 448 + 896;
#define EXP2(x) __builtin_amdgcn_exp2f(x)
#define SB0() __builtin_amdgcn_sched_barrier(0)

DI u32x4 expack(const f32x16& s, int o) {
  u32x4 p;
#pragma unroll
  for (int i = 0; i < 4; ++i) p[i] = pk2(EXP2(s[o + 2 * i]), EXP2(s[o + 2 * i + 1]));
  return p;
}

__global__ __launch_bounds__(512, 2) void k2_attn(char* __restrict__ ws, const u16* __restrict__ wo,
                                                  const u16* __restrict__ wg, const u16* __restrict__ wu,
                                                  const u16* __restrict__ wd) {
  extern __shared__ __attribute__((aligned(16))) char lds[];
  const int tid = threadIdx.x, lane = tid & 63;
  const int w = __builtin_amdgcn_readfirstlane(tid >> 6);
  const int b = blockIdx.x;
  const int tt = b >> 1, hg = b & 1;
  const int qg = w & 3, kh = w >> 2;
  const int c32 = lane & 31, h = lane >> 5;
  STAMP_DECL; STAMP(0);

  glds16(ws + WS_Q + (size_t)(tt * 16 + hg * 8 + w) * 1024 + lane * 16, lds + K2_CMB + w * 1024);
  auto load_kv = [&]() {
#pragma unroll
    for (int c = 0; c < 8; ++c) {
      const int tile = kh * 32 + c * 4 + qg;
      glds16(ws + WS_K + (size_t)tile * 1024 + lane * 16, lds + K2_K + tile * 1024);
      glds16(ws + WS_V + (size_t)tile * 1024 + lane * 16, lds + K2_V + tile * 1024);
    }
  };
  load_kv();
  asm volatile("" ::: "memory");
  const int gw = b * 8 + w;
  const int unit = gw < NUNITS ? gw : NUNITS - 1;
  u32x4 wraw[4];
  int dst_tile, dst_stride;
  const bool isgu = unit >= 576;
  {
    if (!isgu) {
      const u16* src; int rb0, kb;
      if (unit < 128) { kb = unit >> 3; rb0 = (unit & 7) * 4; src = wo; dst_tile = (int)(WS_WO >> 10) + rb0 * 16 + kb; dst_stride = 16; }
      else { const int v = unit - 128; kb = v >> 3; rb0 = (v & 7) * 4; src = wd; dst_tile = (int)(WS_WD >> 10) + rb0 * 56 + kb; dst_stride = 56; }
      src += (size_t)(kb * 32 + (lane >> 3)) * 512 + rb0 * 16 + (lane & 7) * 8;
#pragma unroll
      for (int i = 0; i < 4; ++i) wraw[i] = *(const u32x4*)(src + (size_t)i * 8 * 512);
    } else {
      const int v = unit - 576, kb = v / 56, hq = v % 56;
      dst_tile = (int)(WS_WGU >> 10) + hq * 4 * 16 + kb; dst_stride = 16;
      const size_t off = (size_t)(kb * 32 + (lane >> 2)) * 1792 + hq * 32 + (lane & 3) * 8;
      wraw[0] = *(const u32x4*)(wg + off);
      wraw[1] = *(const u32x4*)(wg + off + 16 * 1792);
      wraw[2] = *(const u32x4*)(wu + off);
      wraw[3] = *(const u32x4*)(wu + off + 16 * 1792);
    }
  }
  asm volatile("" ::: "memory");
  {
    const unsigned ONE2 = 0x3f803f80u;
    if (tid < 256) *(u32x4*)(lds + K2_ONES + tid * 16) = (u32x4){ONE2, ONE2, ONE2, ONE2};
    if (tid == 0) *(unsigned*)(lds + K2_FLAG) = 0u;
  }
  const char* kp = lds + K2_K + kh * 32768 + lane * 16;
  const bool vreal = c32 < 16;
  const int onesoff = (c32 >= 20 && c32 < 28) ? 64 : 0;
  const char* vp = vreal ? (lds + K2_V + kh * 32768 + (h * 16 + c32) * 16) : (lds + K2_ONES + onesoff);
  const int vstep = vreal ? 4096 : 0;

  wait_lgkm0();
  STAMP(1);
  wait_vm<14 + 4>();
  bar();
  STAMP(2);
  const bf16x8 q0 = lds_frag(lds + K2_CMB + (qg * 2) * 1024 + lane * 16), q1 = lds_frag(lds + K2_CMB + (qg * 2 + 1) * 1024 + lane * 16);
  auto retile = [&]() {
    char* tb = lds + (w >> 2) * 65536 + ((w >> 1) & 1) * 32768 + (w & 1) * 4096;
    if (!isgu) {
#pragma unroll
      for (int i = 0; i < 4; ++i) *(u32x4*)(tb + (i * 8 + (lane >> 3)) * 128 + (lane & 7) * 16) = wraw[i];
    } else {
#pragma unroll
      for (int i = 0; i < 4; ++i) {
        char* p = tb + ((i & 1) * 16 + (lane >> 2)) * 128 + (lane & 3) * 32 + (i >> 1) * 4;
#pragma unroll
        for (int e = 0; e < 4; ++e) *(unsigned*)(p + e * 8) = wraw[i][e];
      }
    }
    if (gw < NUNITS) {
      const int g = lane >> 4, q4 = (lane & 15) >> 2, p4 = lane & 3;
      const char* rp = tb + (8 * g + q4) * 128 + p4 * 8;
#pragma unroll
      for (int t = 0; t < 4; ++t) {
        const s16x4 lo = lds_tr(rp + t * 32), hi = lds_tr(rp + t * 32 + 512);
        const bf16x8 f = __builtin_shufflevector(lo, hi, 0, 1, 2, 3, 4, 5, 6, 7);
        st16_wt(ws, (unsigned)(dst_tile + t * dst_stride) * 1024u + lane * 16, __builtin_bit_cast(u32x4, f));
        if (isgu) {
          const u32x4 u = __builtin_bit_cast(u32x4, f);
          float cs = ((bflo(u[0]) + bfhi(u[0])) + (bflo(u[1]) + bfhi(u[1]))) + ((bflo(u[2]) + bfhi(u[2])) + (bflo(u[3]) + bfhi(u[3])));
          cs = xsum_g(cs);
          const int v = unit - 576;
          if (lane < 16) ((float*)(ws + WS_CP))[(v / 56) * 3584 + ((v % 56) * 4 + t) * 16 + lane] = cs;
        }
      }
    }
  };
  f32x16 o0, o1;
  float m0, m1;
  int nmax = 1;
  for (int attempt = 0; attempt < 2; ++attempt) {
    {
      float a0 = -INFINITY, a1 = -INFINITY;
      const f32x16 z = {};
      for (int t = 0; t < nmax; ++t) {
        const bf16x8 kf = lds_frag(kp + t * 1024);
        const f32x16 s0 = MFMA32(kf, q0, z), s1 = MFMA32(kf, q1, z);
#pragma unroll
        for (int i = 0; i < 16; ++i) { a0 = fmaxf(a0, s0[i]); a1 = fmaxf(a1, s1[i]); }
      }
      m0 = fmaxf(a0, __shfl_xor(a0, 32));
      m1 = fmaxf(a1, __shfl_xor(a1, 32));
    }
    f32x16 n0, n1;
#pragma unroll
    for (int i = 0; i < 16; ++i) { n0[i] = -m0; n1[i] = -m1; o0[i] = 0.f; o1[i] = 0.f; }
    asm volatile("" : "+v"(n0), "+v"(n1));
    STAMP(3);
    const char* vq = vp;
    bf16x8 kn = lds_frag(kp);
    f32x16 sa0 = MFMA32(kn, q0, n0), sa1 = MFMA32(kn, q1, n1), sb0, sb1;
    kn = lds_frag(kp + 1024);
    bf16x8 vA0 = lds_frag(vq), vA1 = lds_frag(vq + 512), vB0 = vA0, vB1 = vA1;
    u32x4 pa, pb, pc = {}, pd = {};
#define PAIR(P, i, C, o) P[i] = pk2(EXP2(C[o]), EXP2(C[(o) + 1]))
#define BC(x) __builtin_bit_cast(bf16x8, x)
#define TILE_STEP(C0, C1, N0, N1, VC0, VC1, VP0, VP1, KOFF, VOFF, HASN, HASK)          \
    {                                                                                  \
      o1 = MFMA32(VP0, BC(pc), o1);                                                    \
      PAIR(pa, 0, C0, 0); PAIR(pa, 1, C0, 2); PAIR(pa, 2, C0, 4); SB0();               \
      o1 = MFMA32(VP1, BC(pd), o1);                                                    \
      PAIR(pa, 3, C0, 6); PAIR(pb, 0, C0, 8); PAIR(pb, 1, C0, 10); SB0();              \
      if (HASN) N0 = MFMA32(kn, q0, n0);                                               \
      PAIR(pb, 2, C0, 12); PAIR(pb, 3, C0, 14); SB0();                                 \
      o0 = MFMA32(VC0, BC(pa), o0); SB0();                                             \
      if (HASN) { VP0 = lds_frag(vq + (VOFF)); VP1 = lds_frag(vq + (VOFF) + 512); }    \
      PAIR(pc, 0, C1, 0); PAIR(pc, 1, C1, 2); PAIR(pc, 2, C1, 4); SB0();               \
      if (HASN) N1 = MFMA32(kn, q1, n1);                                               \
      PAIR(pc, 3, C1, 6); PAIR(pd, 0, C1, 8); PAIR(pd, 1, C1, 10); SB0();              \
      o0 = MFMA32(VC1, BC(pb), o0); SB0();                                             \
      if (HASK) kn = lds_frag(kp + (KOFF));                                            \
      PAIR(pd, 2, C1, 12); PAIR(pd, 3, C1, 14); SB0();                                 \
    }
#pragma unroll
    for (int c = 0; c < 8; ++c) {
      TILE_STEP(sa0, sa1, sb0, sb1, vA0, vA1, vB0, vB1, (c * 4 + 2) * 1024, 1024, true, true);
      TILE_STEP(sb0, sb1, sa0, sa1, vB0, vB1, vA0, vA1, (c * 4 + 3) * 1024, 2048, true, true);
      if (c < 3) {
        if (c == 0) wait_vm<12 + 4>(); else if (c == 1) wait_vm<10 + 4>(); else wait_vm<0>();
        bar();
        if (c == 2 && attempt == 0) retile();
      }
      TILE_STEP(sa0, sa1, sb0, sb1, vA0, vA1, vB0, vB1, (c * 4 + 4) * 1024, 3072, true, c < 7);
      vq += vstep;
      TILE_STEP(sb0, sb1, sa0, sa1, vB0, vB1, vA0, vA1, (c * 4 + 5) * 1024, 0, c < 7, c < 7);
    }
    o1 = MFMA32(vB0, BC(pc), o1);
    o1 = MFMA32(vB1, BC(pd), o1);
    STAMP(4);
    const bool ok = (o0[8] < 1e30f) && (o1[8] < 1e30f);
    if (!__all(ok)) { if (lane == 0) *(volatile unsigned*)(lds + K2_FLAG) = 1u; }
    __syncthreads();
    const unsigned f = *(const LDSP volatile unsigned*)(LDSP char*)(lds + K2_FLAG);
    if (f == 0u || attempt == 1) break;
    nmax = 32;
    load_kv();
    wait_vm<0>();
    __syncthreads();
  }
  {
    f32x4* cm = (f32x4*)(lds + K2_CMB + qg * 5120);
    if (kh == 1) {
      cm[0 * 64 + lane] = (f32x4){o0[0], o0[1], o0[2], o0[3]};
      cm[1 * 64 + lane] = (f32x4){o0[4], o0[5], o0[6], o0[7]};
      cm[2 * 64 + lane] = (f32x4){o1[0], o1[1], o1[2], o1[3]};
      cm[3 * 64 + lane] = (f32x4){o1[4], o1[5], o1[6], o1[7]};
      cm[4 * 64 + lane] = (f32x4){m0, o0[8], m1, o1[8]};
    }
    __syncthreads();
    if (kh == 0) {
      const f32x4 st = cm[4 * 64 + lane];
#pragma unroll
      for (int qb = 0; qb < 2; ++qb) {
        const float ma = qb ? m1 : m0, mb = qb ? st[2] : st[0];
        const float la = qb ? o1[8] : o0[8], lb = qb ? st[3] : st[1];
        const float mm = fmaxf(ma, mb);
        const float fa = EXP2(ma - mm), fb = EXP2(mb - mm);
        const float inv = 1.f / (la * fa + lb * fb);
        const float ga = fa * inv, gb = fb * inv;
        const f32x4 b0 = cm[(qb * 2 + 0) * 64 + lane], b1 = cm[(qb * 2 + 1) * 64 + lane];
        float v[8];
#pragma unroll
        for (int i = 0; i < 4; ++i) {
          v[i] = (qb ? o1[i] : o0[i]) * ga + b0[i] * gb;
          v[4 + i] = (qb ? o1[4 + i] : o0[4 + i]) * ga + b1[i] * gb;
        }
        char* dst = ws + WS_O + (size_t)(tt * 16 + hg * 8 + qg * 2 + qb) * 1024 + (((c32 >> 4) * 2) * 16 + (c32 & 15)) * 16 + h * 8;
        *(u32x2*)dst = (u32x2){pk2(v[0], v[1]), pk2(v[2], v[3])};
        *(u32x2*)(dst + 256) = (u32x2){pk2(v[4], v[5]), pk2(v[6], v[7])};
      }
    }
  }
  STAMP(5);
  STAMP(6); STAMP_OUT(1);
}

template <int MODE>
__global__ __launch_bounds__(512, 2) void k35_gemm(char* __restrict__ ws, const u16* __restrict__ x, u16* __restrict__ out) {
  constexpr int NKB = MODE ? 56 : 16, NS = NKB / 4;
  extern __shared__ __attribute__((aligned(16))) char lds[];
  const int tid = threadIdx.x, lane = tid & 63;
  const int w = __builtin_amdgcn_readfirstlane(tid >> 6);
  const int b = blockIdx.x, xcd = b & 7, idx = b >> 3;
  const int tg = xcd * 4 + (idx & 3), ng = idx >> 2;
  const int r = lane & 15, g = lane >> 4;
  const int kg = w >> 2, wn = (w >> 1) & 1, wt = w & 1;
  const int nt = ng * 4 + wn * 2 + kg;
  STAMP_DECL; STAMP(0);
  u32x2 res[2];
#pragma unroll
  for (int t = 0; t < 2; ++t) {
    const int tt = tg * 4 + wt * 2 + t;
    if constexpr (MODE == 0) res[t] = *(const u32x2*)(x + (size_t)(tt * 16 + r) * 512 + nt * 16 + g * 4);
    else res[t] = *(const u32x2*)(ws + WS_H + (size_t)(tt * 16 + (nt >> 1)) * 1024 + (((nt & 1) * 2 + (g >> 1)) * 16 + r) * 16 + (g & 1) * 8);
  }
  float cpart[16];
  if constexpr (MODE == 0) {
    if (b < 224 && tid < 16) {
#pragma unroll
      for (int i = 0; i < 16; ++i) cpart[i] = ((const float*)(ws + WS_CP))[i * 3584 + b * 16 + tid];
    }
  }
  asm volatile("" ::: "memory");
  const char* A = ws + (MODE ? WS_WD : WS_WO) + (size_t)(ng * 4) * NKB * 1024;
  const char* B = ws + (MODE ? WS_HH : WS_O) + (size_t)(tg * 4) * NKB * 1024;
  const char* src = (w < 4 ? A + (size_t)w * NKB * 1024 : B + (size_t)(w - 4) * NKB * 1024) + lane * 16;
  auto issue = [&](int s) {
    char* dst = lds + (s & 3) * 32768 + w * 1024;
#pragma unroll
    for (int i = 0; i < 4; ++i) glds16(src + (size_t)(s * 4 + i) * 1024, dst + i * 8192);
  };
#pragma unroll
  for (int s = 0; s < 4; ++s) issue(s);
  f32x4 acc[2][2] = {};
  bf16x8 af[2][2][2], bf[2][2][2];
  auto rd = [&](int s, int buf) {
    const char* base = lds + (s & 3) * 32768 + lane * 16;
#pragma unroll
    for (int kk = 0; kk < 2; ++kk) {
      const char* p = base + (kg * 2 + kk) * 8192;
#pragma unroll
      for (int i = 0; i < 2; ++i) { af[buf][kk][i] = lds_frag(p + (wn * 2 + i) * 1024); bf[buf][kk][i] = lds_frag(p + (4 + wt * 2 + i) * 1024); }
    }
  };
  wait_vm<12>();
  bar();
  STAMP(1);
  rd(0, 0);
#pragma unroll
  for (int s = 0; s < NS; ++s) {
    if (s + 1 < NS) {
      const int later = (NS - 2 - s) < 2 ? (NS - 2 - s) : 2;
      if (later == 2) wait_vm<8>(); else if (later == 1) wait_vm<4>(); else wait_vm<0>();
      wait_lgkm0();
      bar();
      if (s == NS - 2) STAMP(2);
      if (s + 4 < NS) issue(s + 4);
      rd(s + 1, (s + 1) & 1);
    }
#pragma unroll
    for (int kk = 0; kk < 2; ++kk)
#pragma unroll
      for (int i = 0; i < 2; ++i)
#pragma unroll
        for (int t = 0; t < 2; ++t) acc[i][t] = MFMA16(af[s & 1][kk][i], bf[s & 1][kk][t], acc[i][t]);
  }
  STAMP(3);
  bar();
  f32x4* e4 = (f32x4*)(lds + w * 2048);
  e4[lane] = kg ? acc[0][0] : acc[1][0];
  e4[64 + lane] = kg ? acc[0][1] : acc[1][1];
  __syncthreads();
  const f32x4* o4 = (const f32x4*)(lds + (w ^ 4) * 2048);
  if constexpr (MODE == 0) {
    float* part = (float*)(lds + 16384);
#pragma unroll
    for (int t = 0; t < 2; ++t) {
      const f32x4 a = (t ? (kg ? acc[1][1] : acc[0][1]) : (kg ? acc[1][0] : acc[0][0])) + o4[t * 64 + lane];
      const int tt = tg * 4 + wt * 2 + t;
      const u32x2 xr = res[t];
      const unsigned u0 = pk2(bflo(xr[0]) + a[0], bfhi(xr[0]) + a[1]), u1 = pk2(bflo(xr[1]) + a[2], bfhi(xr[1]) + a[3]);
      char* dst = ws + WS_H + (size_t)(tt * 16 + (nt >> 1)) * 1024 + (((nt & 1) * 2 + (g >> 1)) * 16 + r) * 16 + (g & 1) * 8;
      *(u32x2*)dst = (u32x2){u0, u1};
      const float h0 = bflo(u0), h1 = bfhi(u0), h2 = bflo(u1), h3 = bfhi(u1);
      const float s1 = xsum_g((h0 + h1) + (h2 + h3)), s2 = xsum_g((h0 * h0 + h1 * h1) + (h2 * h2 + h3 * h3));
      if (g == 0) *(f32x2*)(part + ((wn * 2 + kg) * 64 + (wt * 2 + t) * 16 + r) * 2) = (f32x2){s1, s2};
    }
    __syncthreads();
    if (tid < 64) {
      f32x2 s = {0.f, 0.f};
#pragma unroll
      for (int i = 0; i < 4; ++i) s += *(const f32x2*)(part + (i * 64 + tid) * 2);
      *(f32x2*)(ws + WS_ST + ((size_t)(tg * 64 + tid) * 8 + ng) * 8) = s;
    }
    if (b < 224 && tid < 16) {
      float c = 0.f;
#pragma unroll
      for (int i = 0; i < 16; ++i) c += cpart[i];
      ((float*)(ws + WS_CS))[b * 16 + tid] = c;
    }
  } else {
#pragma unroll
    for (int t = 0; t < 2; ++t) {
      const f32x4 a = (t ? (kg ? acc[1][1] : acc[0][1]) : (kg ? acc[1][0] : acc[0][0])) + o4[t * 64 + lane];
      const int tt = tg * 4 + wt * 2 + t;
      const u32x2 hr = res[t];
      *(u32x2*)(out + (size_t)(tt * 16 + r) * 512 + nt * 16 + g * 4) =
          (u32x2){pk2(bflo(hr[0]) + a[0], bfhi(hr[0]) + a[1]), pk2(bflo(hr[1]) + a[2], bfhi(hr[1]) + a[3])};
    }
  }
  STAMP(4); STAMP_OUT(MODE ? 4 : 2);
}

constexpr int K4_SLOT = 22 * 1024, K4_NSLOT = 7, K4_ST = K4_SLOT * K4_NSLOT, K4_LDS = K4_ST + 1024;

template <int NP> DI void k4_wait(int later) {
  if (later >= 6) wait_vm<6 * NP>(); else if (later == 5) wait_vm<5 * NP>(); else if (later == 4) wait_vm<4 * NP>();
  else if (later == 3) wait_vm<3 * NP>(); else if (later == 2) wait_vm<2 * NP>(); else if (later == 1) wait_vm<NP>(); else wait_vm<0>();
}

template <int NT, int NP>
DI void k4_body(char* __restrict__ ws, char* lds, int w, int tid, int tg, int hgp, int tile0, int wt) {
  const int lane = tid & 63;
  const int r = lane & 15, g = lane >> 4;
  STAMP_DECL; STAMP(0);
  f32x4 sp[4];
  if (NT == 4 && w < 2) {
    const char* p = ws + WS_ST + (size_t)(tg * 128 + tid) * 64;
    asm volatile("global_load_dwordx4 %0, %4, off\n\tglobal_load_dwordx4 %1, %4, off offset:16\n\t"
                 "global_load_dwordx4 %2, %4, off offset:32\n\tglobal_load_dwordx4 %3, %4, off offset:48"
                 : "=&v"(sp[0]), "=&v"(sp[1]), "=&v"(sp[2]), "=&v"(sp[3]) : "v"(p) : "memory");
  }
  f32x4 c4[NT];
#pragma unroll
  for (int i = 0; i < NT; ++i) c4[i] = *(const f32x4*)(ws + WS_CS + (size_t)((hgp * 14 + tile0 + i) * 16 + g * 4) * 4);
  asm volatile("" ::: "memory");
  const char* A = ws + WS_WGU + (size_t)(hgp * 14) * 16 * 1024 + lane * 16;
  const char* B = ws + WS_H + (size_t)(tg * 8) * 16 * 1024 + lane * 16;
  const char* s0 = A + (size_t)w * 16384;
  const char* s1 = (w < 6) ? A + (size_t)(w + 8) * 16384 : B + (size_t)(w - 6) * 16384;
  const char* s2 = B + (size_t)(w + 2) * 16384;
  auto issue = [&](int kb, int slot) {
    char* dst = lds + slot * K4_SLOT + w * 1024;
    glds16(s0 + kb * 1024, dst);
    glds16(s1 + kb * 1024, dst + 8192);
    if (NP == 3) glds16(s2 + kb * 1024, dst + 16384);
  };
#pragma unroll
  for (int s = 0; s < 7; ++s) issue(s, s);
  f32x4 acc[NT][4] = {};
  bf16x8 bf[2][4], af[2][NT];
  auto rd = [&](int s, int buf) {
    const char* base = lds + (s % K4_NSLOT) * K4_SLOT + lane * 16;
#pragma unroll
    for (int t = 0; t < 4; ++t) bf[buf][t] = lds_frag(base + (14 + wt * 4 + t) * 1024);
#pragma unroll
    for (int i = 0; i < NT; ++i) af[buf][i] = lds_frag(base + (tile0 + i) * 1024);
  };
  k4_wait<NP>(6);
  if (NT == 4 && w < 2) {
    asm volatile("" : "+v"(sp[0]), "+v"(sp[1]), "+v"(sp[2]), "+v"(sp[3]));
    const f32x4 v = (sp[0] + sp[1]) + (sp[2] + sp[3]);
    const float mean = (v[0] + v[2]) * (1.f / 512.f);
    const float var = fmaxf((v[1] + v[3]) * (1.f / 512.f) - mean * mean, 0.f);
    *(f32x2*)(lds + K4_ST + tid * 8) = (f32x2){mean, rsqrtf(var + EPS)};
  }
  bar();
  STAMP(1);
  rd(0, 0);
#pragma unroll
  for (int s = 0; s < 16; ++s) {
    if (s + 1 < 16) {
      k4_wait<NP>((14 - s) < 5 ? (14 - s) : 5);
      wait_lgkm0();
      bar();
      if (s == 14) STAMP(2);
      rd(s + 1, (s + 1) & 1);
    }
    SB0();
#pragma unroll
    for (int i = 0; i < NT; ++i) {
#pragma unroll
      for (int t = 0; t < 4; ++t) acc[i][t] = MFMA16(af[s & 1][i], bf[s & 1][t], acc[i][t]);
      if (s + 7 < 16 && s + 1 < 16) {
        char* dst = lds + (s % K4_NSLOT) * K4_SLOT + w * 1024;
        if (i == 0) glds16(s0 + (s + 7) * 1024, dst);
        if (i == 1) glds16(s1 + (s + 7) * 1024, dst + 8192);
        if (i == 2 && NP == 3) glds16(s2 + (s + 7) * 1024, dst + 16384);
      }
      SB0();
    }
    SB0();
  }
  STAMP(3);
  const f32x2* st = (const f32x2*)(lds + K4_ST);
  f32x2 ms[4];
#pragma unroll
  for (int t = 0; t < 4; ++t) ms[t] = st[(wt * 4 + t) * 16 + r];
#pragma unroll
  for (int i = 0; i < NT; ++i) {
    const int T = hgp * 14 + tile0 + i;
#pragma unroll
    for (int t = 0; t < 4; ++t) {
      const f32x4 a = acc[i][t];
      float o[2];
#pragma unroll
      for (int e = 0; e < 2; ++e) {
        const float gv = (a[e] - ms[t][0] * c4[i][e]) * ms[t][1];
        const float uv = (a[2 + e] - ms[t][0] * c4[i][2 + e]) * ms[t][1];
        const float z = gv * (1.f + 0.044715f * gv * gv) * (-2.f * 0.7978845608028654f * 1.4426950408889634f);
        const float sg = __builtin_amdgcn_rcpf(1.f + EXP2(z));
        o[e] = gv * sg * uv;
      }
      const int tt = tg * 8 + wt * 4 + t;
      *(unsigned*)(ws + WS_HH + (size_t)(tt * 56 + (T >> 2)) * 1024 + ((T & 3) * 16 + r) * 16 + g * 4) = pk2(o[0], o[1]);
    }
  }
  STAMP(4); STAMP_OUT(3);
}

__global__ __launch_bounds__(512, 2) void k4_gateup(char* __restrict__ ws) {
  extern __shared__ __attribute__((aligned(16))) char lds[];
  const int tid = threadIdx.x;
  const int w = __builtin_amdgcn_readfirstlane(tid >> 6);
  const int b = blockIdx.x, xcd = b & 7, idx = b >> 3;
  const int tg = 8 * (xcd & 1) + (idx & 7), hgp = 4 * (xcd >> 1) + (idx >> 3);
  const int wt = w & 1;
  if (w < 4) k4_body<4, 3>(ws, lds, w, tid, tg, hgp, (w >> 1) * 7, wt);
  else if (w < 6) k4_body<3, 3>(ws, lds, w, tid, tg, hgp, 4, wt);
  else k4_body<3, 2>(ws, lds, w, tid, tg, hgp, 11, wt);
}

extern "C" void kernel_launch(void* const* d_in, const int* in_sizes, int n_in, void* d_out, int out_size,
                              void* d_ws, size_t ws_size, hipStream_t stream) {
  const u16* x = (const u16*)d_in[0];
  const u16* wq = (const u16*)d_in[1];
  const u16* wk = (const u16*)d_in[2];
  const u16* wv = (const u16*)d_in[3];
  const u16* wo = (const u16*)d_in[4];
  const u16* wg = (const u16*)d_in[6];
  const u16* wu = (const u16*)d_in[7];
  const u16* wd = (const u16*)d_in[8];
  char* ws = (char*)d_ws;
  (void)hipFuncSetAttribute((const void*)k1_qkv, hipFuncAttributeMaxDynamicSharedMemorySize, K1_LDS);
  (void)hipFuncSetAttribute((const void*)k2_attn, hipFuncAttributeMaxDynamicSharedMemorySize, K2_LDS);
  (void)hipFuncSetAttribute((const void*)k35_gemm<0>, hipFuncAttributeMaxDynamicSharedMemorySize, 131072);
  (void)hipFuncSetAttribute((const void*)k35_gemm<1>, hipFuncAttributeMaxDynamicSharedMemorySize, 131072);
  (void)hipFuncSetAttribute((const void*)k4_gateup, hipFuncAttributeMaxDynamicSharedMemorySize, K4_LDS);
  k1_qkv<<<256, 512, K1_LDS, stream>>>(x, wq, wk, wv, ws);
  k2_attn<<<256, 512, K2_LDS, stream>>>(ws, wo, wg, wu, wd);
  k35_gemm<0><<<256, 512, 131072, stream>>>(ws, x, nullptr);
  k4_gateup<<<256, 512, K4_LDS, stream>>>(ws);
  k35_gemm<1><<<256, 512, 131072, stream>>>(ws, x, (u16*)d_out);
}
```

```cpp
#include <hip/hip_runtime.h>
#include <hip/hip_bf16.h>
#include <stdint.h>

typedef __attribute__((ext_vector_type(8))) short bf16x8;
typedef __attribute__((ext_vector_type(4))) short s16x4;
typedef __attribute__((ext_vector_type(4))) float f32x4;
typedef __attribute__((ext_vector_type(2))) float f32x2;
typedef __attribute__((ext_vector_type(16))) float f32x16;
typedef __attribute__((ext_vector_type(4))) unsigned u32x4;
typedef __attribute__((ext_vector_type(2))) unsigned u32x2;
typedef __attribute__((ext_vector_type(2))) __bf16 bf16x2_t;
typedef unsigned short u16;
typedef unsigned long long u64;
typedef __attribute__((address_space(1))) unsigned long long gu64;
typedef __attribute__((address_space(1))) unsigned gu32;
#define DI __device__ __forceinline__
#define LDSP __attribute__((address_space(3)))
#define RLX_AGENT __ATOMIC_RELAXED, __HIP_MEMORY_SCOPE_AGENT

#define NLAUNCH 1
constexpr bool FUSED = (NLAUNCH == 1);

constexpr float QSCALE = 0.25f * 1.4426950408889634f;
constexpr float EPS = 1e-5f;

constexpr size_t WS_Q = 0;
constexpr size_t WS_O = WS_Q + 2097152;
constexpr size_t WS_H = WS_O + 2097152;
constexpr size_t WS_HH = WS_H + 2097152;
constexpr size_t WS_K = WS_HH + 7340032;
constexpr size_t WS_V = WS_K + 65536;
constexpr size_t WS_WO = WS_V + 65536;
constexpr size_t WS_WGU = WS_WO + 524288;
constexpr size_t WS_WD = WS_WGU + 3670016;
constexpr size_t WS_CP = WS_WD + 1835008;
constexpr size_t WS_ST = WS_CP + 16 * 3584 * 4;
constexpr size_t WS_FL = WS_ST + 2048 * 64;
constexpr size_t WS_DBG = 64u << 20;
constexpr int FL_A = 256, FL_R = FL_A + 2048, FL_B = FL_R + 2048, FL_C = FL_B + 2048, FL_D = FL_C + 2048;

#define STAMP_DECL
#define STAMP(i) do {} while (0)
#define STAMP_OUT(kid) do {} while (0)

DI unsigned pk2(float a, float b) {
  f32x2 v = {a, b};
  return __builtin_bit_cast(unsigned, __builtin_convertvector(v, bf16x2_t));
}
DI float bflo(unsigned u) { return __uint_as_float(u << 16); }
DI float bfhi(unsigned u) { return __uint_as_float(u & 0xffff0000u); }
DI float rbf(float a) { return bflo(pk2(a, a)); }
DI void glds16(const void* g, char* l) { __builtin_amdgcn_global_load_lds((const unsigned*)g, (unsigned*)l, 16, 0, 0); }
DI void glds16s(const void* g, char* l) { __builtin_amdgcn_global_load_lds((const unsigned*)g, (unsigned*)l, 16, 0, 16); }
DI void st16s(char* base, unsigned off, u32x4 v) {
  const __amdgpu_buffer_rsrc_t rs = __builtin_amdgcn_make_buffer_rsrc((void*)base, (short)0, 0x7fffffff, 0x00020000);
  __builtin_amdgcn_raw_buffer_store_b128(v, rs, off, 0, 16);
}
DI void st8s(char* p, u32x2 v) { __hip_atomic_store((gu64*)p, __builtin_bit_cast(u64, v), RLX_AGENT); }
DI void st4s(char* p, float v) { __hip_atomic_store((gu32*)p, __float_as_uint(v), RLX_AGENT); }
DI u32x2 ld8s(const char* p) { return __builtin_bit_cast(u32x2, __hip_atomic_load((gu64*)p, RLX_AGENT)); }
DI float ld4s(const char* p) { return __uint_as_float(__hip_atomic_load((gu32*)p, RLX_AGENT)); }
template <int N> DI void wait_vm() { asm volatile("s_waitcnt vmcnt(%0)" ::"n"(N) : "memory"); }
DI void wait_lgkm0() { __builtin_amdgcn_s_waitcnt(0xC07F); asm volatile("" ::: "memory"); }
DI void bar() { asm volatile("" ::: "memory"); __builtin_amdgcn_s_barrier(); asm volatile("" ::: "memory"); }
DI void lbar() { wait_lgkm0(); bar(); }
DI bf16x8 lds_frag(const char* p) { return *(const bf16x8*)p; }
DI s16x4 lds_tr(const char* p) {
  return __builtin_bit_cast(s16x4, __builtin_amdgcn_ds_read_tr16_b64_v4i16((LDSP s16x4*)(LDSP char*)p));
}
#define MFMA16(a, b, c) __builtin_amdgcn_mfma_f32_16x16x32_bf16(a, b, c, 0, 0, 0)
#define MFMA32(a, b, c) __builtin_amdgcn_mfma_f32_32x32x16_bf16(a, b, c, 0, 0, 0)
#define EXP2(x) __builtin_amdgcn_exp2f(x)
#define SB0() __builtin_amdgcn_sched_barrier(0)
DI float xsum_g(float v) {
  v += __shfl_xor(v, 16);
  v += __shfl_xor(v, 32);
  return v;
}

DI void publish(char* ws, int fl, int slot, u64 tag) {
  if constexpr (FUSED) {
    asm volatile("s_waitcnt vmcnt(0)" ::: "memory");
    bar();
    if (threadIdx.x == 0) __hip_atomic_store((gu64*)(ws + WS_FL + fl + slot * 8), tag, RLX_AGENT);
  }
}
template <int N> DI void poll(char* ws, int fl, int slot0, u64 tag, int lane) {
  if constexpr (FUSED) {
    for (unsigned spins = 0; spins < (1u << 21); ++spins) {
      bool ok = true;
      if constexpr (N <= 64) {
        if (lane < N) ok = __hip_atomic_load((gu64*)(ws + WS_FL + fl + (slot0 + lane) * 8), RLX_AGENT) == tag;
      } else {
#pragma unroll
        for (int i = 0; i < N / 64; ++i) ok &= __hip_atomic_load((gu64*)(ws + WS_FL + fl + (slot0 + i * 64 + lane) * 8), RLX_AGENT) == tag;
      }
      if (__all(ok)) break;
      __builtin_amdgcn_s_sleep(1);
    }
    asm volatile("" ::: "memory");
  }
}

constexpr int NUNITS = 128 + 448 + 896;

struct WUnit { u32x4 raw[4]; int dst_tile, dst_stride, unit; };
DI void wunit_load(WUnit& u, const u16* __restrict__ wo, const u16* __restrict__ wg, const u16* __restrict__ wu,
                   const u16* __restrict__ wd, int gw, int lane) {
  const int unit = gw < NUNITS ? gw : NUNITS - 1;
  u.unit = unit;
  if (unit < 576) {
    const u16* src; int rb0, kb;
    if (unit < 128) { kb = unit >> 3; rb0 = (unit & 7) * 4; src = wo; u.dst_tile = (int)(WS_WO >> 10) + rb0 * 16 + kb; u.dst_stride = 16; }
    else { const int v = unit - 128; kb = v >> 3; rb0 = (v & 7) * 4; src = wd; u.dst_tile = (int)(WS_WD >> 10) + rb0 * 56 + kb; u.dst_stride = 56; }
    src += (size_t)(kb * 32 + (lane >> 3)) * 512 + rb0 * 16 + (lane & 7) * 8;
#pragma unroll
    for (int i = 0; i < 4; ++i) u.raw[i] = *(const u32x4*)(src + (size_t)i * 8 * 512);
  } else {
    const int v = unit - 576, kb = v / 56, hq = v % 56;
    u.dst_tile = (int)(WS_WGU >> 10) + hq * 4 * 16 + kb; u.dst_stride = 16;
    const size_t off = (size_t)(kb * 32 + (lane >> 2)) * 1792 + hq * 32 + (lane & 3) * 8;
    u.raw[0] = *(const u32x4*)(wg + off);
    u.raw[1] = *(const u32x4*)(wg + off + 16 * 1792);
    u.raw[2] = *(const u32x4*)(wu + off);
    u.raw[3] = *(const u32x4*)(wu + off + 16 * 1792);
  }
}

DI void wunit_store(const WUnit& u, char* __restrict__ ws, char* tb, int gw, int lane) {
  const bool isgu = u.unit >= 576;
  if (!isgu) {
#pragma unroll
    for (int i = 0; i < 4; ++i) *(u32x4*)(tb + (i * 8 + (lane >> 3)) * 128 + (lane & 7) * 16) = u.raw[i];
  } else {
#pragma unroll
    for (int i = 0; i < 4; ++i) {
      char* p = tb + ((i & 1) * 16 + (lane >> 2)) * 128 + (lane & 3) * 32 + (i >> 1) * 4;
#pragma unroll
      for (int e = 0; e < 4; ++e) *(unsigned*)(p + e * 8) = u.raw[i][e];
    }
  }
  if (gw < NUNITS) {
    const int g = lane >> 4, q4 = (lane & 15) >> 2, p4 = lane & 3;
    const char* rp = tb + (8 * g + q4) * 128 + p4 * 8;
#pragma unroll
    for (int t = 0; t < 4; ++t) {
      const s16x4 lo = lds_tr(rp + t * 32), hi = lds_tr(rp + t * 32 + 512);
      const bf16x8 f = __builtin_shufflevector(lo, hi, 0, 1, 2, 3, 4, 5, 6, 7);
      st16s(ws, (unsigned)(u.dst_tile + t * u.dst_stride) * 1024u + lane * 16, __builtin_bit_cast(u32x4, f));
      if (isgu) {
        const u32x4 v4 = __builtin_bit_cast(u32x4, f);
        float cs = ((bflo(v4[0]) + bfhi(v4[0])) + (bflo(v4[1]) + bfhi(v4[1]))) + ((bflo(v4[2]) + bfhi(v4[2])) + (bflo(v4[3]) + bfhi(v4[3])));
        cs = xsum_g(cs);
        const int v = u.unit - 576;
        if (lane < 16) st4s(ws + WS_CP + (size_t)((v / 56) * 3584 + ((v % 56) * 4 + t) * 16 + lane) * 4, cs);
      }
    }
  }
}

constexpr int K1_X = 0, K1_W = 65536, K1_KV = 131072, K1_ST = 147456, K1_P = K1_ST + 1024;
constexpr int K2_K = 0, K2_V = 65536, K2_ONES = 131072, K2_CMB = K2_ONES + 4096, K2_FLAG = K2_CMB + 4 * 5 * 1024;

DI void phaseAB(const u16* __restrict__ x, const u16* __restrict__ wq, const u16* __restrict__ wk, const u16* __restrict__ wv,
                const u16* __restrict__ wo, const u16* __restrict__ wg, const u16* __restrict__ wu, const u16* __restrict__ wd,
                char* __restrict__ ws, char* lds, int tid, int w, u64 tag) {
  const int lane = tid & 63;
  const int b = blockIdx.x, xcd = b & 7, idx = b >> 3;
  const int tg = xcd * 4 + (idx & 3), j = idx >> 2;
  const int r = lane & 15, g = lane >> 4;
  const u16* wkv = (j & 1) ? wv : wk;
  const int tq = j >> 1;
  const int gw = b * 8 + w;
  STAMP_DECL; STAMP(0);
  {
    const int s = lane >> 4, pr = (lane >> 1) & 7, hh = lane & 1;
#pragma unroll
    for (int kq = 0; kq < 8; ++kq) {
      const int row = w * 8 + (lane >> 3), ch = (lane & 7) ^ (lane >> 3);
      glds16(x + (size_t)(tg * 64 + row) * 512 + kq * 64 + ch * 8, lds + K1_X + kq * 8192 + w * 1024);
    }
#pragma unroll
    for (int c = 0; c < 2; ++c) {
      const int kb8b = c * 32 + w * 4, kb8 = kb8b + s;
      const int k = 8 * kb8 + (pr ^ (4 * (kb8 & 1)));
      glds16(wkv + (size_t)k * 16 + hh * 8, lds + K1_KV + kb8b * 256);
    }
  }
  WUnit wun;
  auto load_w = [&]() {
    const int s = lane >> 4, pr = (lane >> 1) & 7, hh = lane & 1;
#pragma unroll
    for (int c = 0; c < 8; ++c) {
      const int kb8 = c * 8 + w;
      const int k = 8 * kb8 + (pr ^ (4 * (kb8 & 1)));
      glds16(wq + (size_t)k * 512 + j * 64 + s * 16 + hh * 8, lds + K1_W + kb8 * 1024);
    }
    asm volatile("" ::: "memory");
    wunit_load(wun, wo, wg, wu, wd, gw, lane);
    asm volatile("" ::: "memory");
  };
  if (w != 0) load_w();
  const unsigned ONE2 = 0x3f803f80u;
  const u32x4 ones4 = {ONE2, ONE2, ONE2, ONE2};
  auto xfrag = [&](int tt, int kb) {
    return lds_frag(lds + K1_X + (kb >> 1) * 8192 + (tt * 16 + r) * 128 + ((((kb & 1) * 4 + g) ^ (r & 7)) * 16));
  };
  if (w != 0) wait_vm<2 + 12>(); else wait_vm<2>();
  bar();
  STAMP(1);
  {
    const bf16x8 ones = __builtin_bit_cast(bf16x8, ones4);
    f32x4 sm = {}, sq = {};
#pragma unroll
    for (int kk = 0; kk < 8; ++kk) {
      const bf16x8 f = xfrag(w & 3, (w >> 2) * 8 + kk);
      sm = MFMA16(ones, f, sm);
      sq = MFMA16(f, f, sq);
    }
    const float d = (r & 2) ? ((r & 1) ? sq[3] : sq[2]) : ((r & 1) ? sq[1] : sq[0]);
    float* stp = (float*)(lds + K1_ST) + ((w >> 2) * 64 + (w & 3) * 16 + r) * 2;
    if (g == 0) stp[0] = sm[0];
    if (g == (r >> 2)) stp[1] = d;
  }
  lbar();
  {
    const int row = tid >> 3;
    const f32x2 s0 = *(const f32x2*)(lds + K1_ST + row * 8), s1 = *(const f32x2*)(lds + K1_ST + 512 + row * 8);
    const float mean = (s0[0] + s1[0]) * (1.f / 512.f);
    const float var = fmaxf((s0[1] + s1[1]) * (1.f / 512.f) - mean * mean, 0.f);
    const float mb = rbf(mean);
    const float sd = rbf(sqrtf(rbf(rbf(var) + EPS)));
    const float inv = 1.f / sd;
    const f32x2 mb2 = {mb, mb}, inv2 = {inv, inv};
    char* p = lds + K1_X + row * 128 + (tid & 7) * 16;
#pragma unroll
    for (int kq = 0; kq < 8; ++kq) {
      u32x4 v = *(const u32x4*)(p + kq * 8192);
#pragma unroll
      for (int e = 0; e < 4; ++e) {
        const f32x2 a = (f32x2){bflo(v[e]), bfhi(v[e])} - mb2;
        const unsigned d = pk2(a[0], a[1]);
        const f32x2 y = (f32x2){bflo(d), bfhi(d)} * inv2;
        v[e] = pk2(y[0], y[1]);
      }
      *(u32x4*)(p + kq * 8192) = v;
    }
  }
  const int q4 = (lane & 15) >> 2, p4 = lane & 3;
  const int troff0 = ((q4) ^ (4 * (g & 1))) * 32 + p4 * 8;
  const int troff1 = ((4 + q4) ^ (4 * (g & 1))) * 32 + p4 * 8;
  if (w != 0) wait_vm<12>(); else wait_vm<0>();
  lbar();
  STAMP(2);
  {
    f32x4 kv = {};
#pragma unroll
    for (int kk = 0; kk < 2; ++kk) {
      const int kb = w * 2 + kk;
      const char* base = lds + K1_KV + (kb * 4 + g) * 256;
      s16x4 lo = lds_tr(base + troff0), hi = lds_tr(base + troff1);
      const bf16x8 kf = __builtin_shufflevector(lo, hi, 0, 1, 2, 3, 4, 5, 6, 7);
      const bf16x8 xx = xfrag(tq, kb);
      if (j & 1) kv = MFMA16(xx, kf, kv);
      else       kv = MFMA16(kf, xx, kv);
    }
    *(f32x4*)(lds + K1_P + w * 1024 + lane * 16) = kv;
  }
  lbar();
  if (w == 0) {
    f32x4 a = {};
#pragma unroll
    for (int i = 0; i < 8; ++i) a += *(const f32x4*)(lds + K1_P + i * 1024 + lane * 16);
    const int tok0 = tg * 64 + tq * 16;
    char* dst;
    if (j & 1) dst = ws + WS_V + (size_t)(tok0 >> 4) * 512 + ((g & 1) * 16 + r) * 16 + (g >> 1) * 8;
    else { const int key = tok0 + r; dst = ws + WS_K + (size_t)(key >> 5) * 1024 + ((g >> 1) * 32 + (key & 31)) * 16 + (g & 1) * 8; }
    st8s(dst, (u32x2){pk2(a[0], a[1]), pk2(a[2], a[3])});
    if constexpr (FUSED) {
      asm volatile("s_waitcnt vmcnt(0)" ::: "memory");
      if (lane == 0) __hip_atomic_store((gu64*)(ws + WS_FL + FL_A + b * 8), tag, RLX_AGENT);
    }
    load_w();
  }
  STAMP(3);
  const int kg = w >> 2, wn = (w >> 1) & 1, wt = w & 1;
  {
    f32x4 acc[2][2] = {};
#pragma unroll
    for (int c = 0; c < 4; ++c) {
      if (c == 0) wait_vm<6 + 4>(); else if (c == 1) wait_vm<4 + 4>(); else if (c == 2) wait_vm<2 + 4>(); else wait_vm<4>();
      bar();
#pragma unroll
      for (int kk = 0; kk < 2; ++kk) {
        const int kb = c * 4 + kg * 2 + kk;
        bf16x8 xf[2], wf[2];
#pragma unroll
        for (int t = 0; t < 2; ++t) xf[t] = xfrag(wt * 2 + t, kb);
#pragma unroll
        for (int i = 0; i < 2; ++i) {
          const char* base = lds + K1_W + (kb * 4 + g) * 1024 + (wn * 2 + i) * 256;
          s16x4 lo = lds_tr(base + troff0), hi = lds_tr(base + troff1);
          wf[i] = __builtin_shufflevector(lo, hi, 0, 1, 2, 3, 4, 5, 6, 7);
        }
#pragma unroll
        for (int i = 0; i < 2; ++i)
#pragma unroll
          for (int t = 0; t < 2; ++t) acc[i][t] = MFMA16(wf[i], xf[t], acc[i][t]);
      }
    }
    lbar();
    f32x4* e4 = (f32x4*)(lds + w * 2048);
    e4[0 * 64 + lane] = kg ? acc[0][0] : acc[1][0];
    e4[1 * 64 + lane] = kg ? acc[0][1] : acc[1][1];
    lbar();
    const f32x4* o4 = (const f32x4*)(lds + (w ^ 4) * 2048);
#pragma unroll
    for (int t = 0; t < 2; ++t) {
      const f32x4 a = (t ? (kg ? acc[1][1] : acc[0][1]) : (kg ? acc[1][0] : acc[0][0])) + o4[t * 64 + lane];
      char* dst = lds + K2_CMB + ((wt * 2 + t) * 2 + wn) * 1024 + ((g >> 1) * 32 + kg * 16 + r) * 16 + (g & 1) * 8;
      *(u32x2*)dst = (u32x2){pk2(a[0] * QSCALE, a[1] * QSCALE), pk2(a[2] * QSCALE, a[3] * QSCALE)};
    }
  }
  if (tid < 256) *(u32x4*)(lds + K2_ONES + tid * 16) = ones4;
  if (tid == 0) *(unsigned*)(lds + K2_FLAG) = 0u;
  STAMP(4);
  wunit_store(wun, ws, lds + 65536 + w * 4096, gw, lane);
  publish(ws, FL_R, b, tag);
  if (w == 0) poll<256>(ws, FL_A, 0, tag, lane);
  lbar();
  if (FUSED && b == 0 && tid == 0) __hip_atomic_store((gu64*)(ws + WS_FL), tag, RLX_AGENT);
  STAMP(5); STAMP_OUT(0);
}

DI void phaseB(char* __restrict__ ws, char* lds, int tid, int w) {
  const int lane = tid & 63;
  const int b = blockIdx.x, xcd = b & 7, idx = b >> 3;
  const int tg = xcd * 4 + (idx & 3), j = idx >> 2;
  const int qg = w & 3, kh = w >> 2;
  const int c32 = lane & 31, h = lane >> 5;
  STAMP_DECL; STAMP(0);
#pragma unroll
  for (int c = 0; c < 8; ++c) {
    const int tile = kh * 32 + c * 4 + qg;
    glds16s(ws + WS_K + (size_t)tile * 1024 + lane * 16, lds + K2_K + tile * 1024);
    glds16s(ws + WS_V + (size_t)tile * 1024 + lane * 16, lds + K2_V + tile * 1024);
  }
  const char* kp = lds + K2_K + kh * 32768 + lane * 16;
  const bool vreal = c32 < 16;
  const int onesoff = (c32 >= 20 && c32 < 28) ? 64 : 0;
  const char* vp = vreal ? (lds + K2_V + kh * 32768 + (h * 16 + c32) * 16) : (lds + K2_ONES + onesoff);
  const int vstep = vreal ? 4096 : 0;
  const bf16x8 q0 = lds_frag(lds + K2_CMB + (qg * 2) * 1024 + lane * 16), q1 = lds_frag(lds + K2_CMB + (qg * 2 + 1) * 1024 + lane * 16);
  wait_vm<14>();
  lbar();
  STAMP(2);
  f32x16 o0, o1;
  float m0, m1;
  int nmax = 1;
  for (int attempt = 0; attempt < 2; ++attempt) {
    {
      float a0 = -INFINITY, a1 = -INFINITY;
      const f32x16 z = {};
      for (int t = 0; t < nmax; ++t) {
        const bf16x8 kf = lds_frag(kp + t * 1024);
        const f32x16 s0 = MFMA32(kf, q0, z), s1 = MFMA32(kf, q1, z);
#pragma unroll
        for (int i = 0; i < 16; ++i) { a0 = fmaxf(a0, s0[i]); a1 = fmaxf(a1, s1[i]); }
      }
      m0 = fmaxf(a0, __shfl_xor(a0, 32));
      m1 = fmaxf(a1, __shfl_xor(a1, 32));
    }
    f32x16 n0, n1;
#pragma unroll
    for (int i = 0; i < 16; ++i) { n0[i] = -m0; n1[i] = -m1; o0[i] = 0.f; o1[i] = 0.f; }
    asm volatile("" : "+v"(n0), "+v"(n1));
    STAMP(3);
    const char* vq = vp;
    bf16x8 kn = lds_frag(kp);
    f32x16 sa0 = MFMA32(kn, q0, n0), sa1 = MFMA32(kn, q1, n1), sb0, sb1;
    kn = lds_frag(kp + 1024);
    bf16x8 vA0 = lds_frag(vq), vA1 = lds_frag(vq + 512), vB0 = vA0, vB1 = vA1;
    u32x4 pa, pb, pc = {}, pd = {};
#define PAIR(P, i, C, o) P[i] = pk2(EXP2(C[o]), EXP2(C[(o) + 1]))
#define BC(x) __builtin_bit_cast(bf16x8, x)
#define TILE_STEP(C0, C1, N0, N1, VC0, VC1, VP0, VP1, KOFF, VOFF, HASN, HASK)          \
    {                                                                                  \
      o1 = MFMA32(VP0, BC(pc), o1);                                                    \
      PAIR(pa, 0, C0, 0); PAIR(pa, 1, C0, 2); PAIR(pa, 2, C0, 4); SB0();               \
      o1 = MFMA32(VP1, BC(pd), o1);                                                    \
      PAIR(pa, 3, C0, 6); PAIR(pb, 0, C0, 8); PAIR(pb, 1, C0, 10); SB0();              \
      if (HASN) N0 = MFMA32(kn, q0, n0);                                               \
      PAIR(pb, 2, C0, 12); PAIR(pb, 3, C0, 14); SB0();                                 \
      o0 = MFMA32(VC0, BC(pa), o0); SB0();                                             \
      if (HASN) { VP0 = lds_frag(vq + (VOFF)); VP1 = lds_frag(vq + (VOFF) + 512); }    \
      PAIR(pc, 0, C1, 0); PAIR(pc, 1, C1, 2); PAIR(pc, 2, C1, 4); SB0();               \
      if (HASN) N1 = MFMA32(kn, q1, n1);                                               \
      PAIR(pc, 3, C1, 6); PAIR(pd, 0, C1, 8); PAIR(pd, 1, C1, 10); SB0();              \
      o0 = MFMA32(VC1, BC(pb), o0); SB0();                                             \
      if (HASK) kn = lds_frag(kp + (KOFF));                                            \
      PAIR(pd, 2, C1, 12); PAIR(pd, 3, C1, 14); SB0();                                 \
    }
#pragma unroll
    for (int c = 0; c < 8; ++c) {
      TILE_STEP(sa0, sa1, sb0, sb1, vA0, vA1, vB0, vB1, (c * 4 + 2) * 1024, 1024, true, true);
      TILE_STEP(sb0, sb1, sa0, sa1, vB0, vB1, vA0, vA1, (c * 4 + 3) * 1024, 2048, true, true);
      if (c < 7) {
        if (c == 0) wait_vm<12>(); else if (c == 1) wait_vm<10>(); else if (c == 2) wait_vm<8>();
        else if (c == 3) wait_vm<6>(); else if (c == 4) wait_vm<4>(); else if (c == 5) wait_vm<2>(); else wait_vm<0>();
        bar();
      }
      TILE_STEP(sa0, sa1, sb0, sb1, vA0, vA1, vB0, vB1, (c * 4 + 4) * 1024, 3072, true, c < 7);
      vq += vstep;
      TILE_STEP(sb0, sb1, sa0, sa1, vB0, vB1, vA0, vA1, (c * 4 + 5) * 1024, 0, c < 7, c < 7);
    }
    o1 = MFMA32(vB0, BC(pc), o1);
    o1 = MFMA32(vB1, BC(pd), o1);
    STAMP(4);
    const bool ok = (o0[8] < 1e30f) && (o1[8] < 1e30f);
    if (!__all(ok)) { if (lane == 0) *(LDSP volatile unsigned*)(LDSP char*)(lds + K2_FLAG) = 1u; }
    lbar();
    const unsigned f = *(const LDSP volatile unsigned*)(LDSP char*)(lds + K2_FLAG);
    if (f == 0u || attempt == 1) break;
    nmax = 32;
  }
  {
    f32x4* cm = (f32x4*)(lds + K2_CMB + qg * 5120);
    if (kh == 1) {
      cm[0 * 64 + lane] = (f32x4){o0[0], o0[1], o0[2], o0[3]};
      cm[1 * 64 + lane] = (f32x4){o0[4], o0[5], o0[6], o0[7]};
      cm[2 * 64 + lane] = (f32x4){o1[0], o1[1], o1[2], o1[3]};
      cm[3 * 64 + lane] = (f32x4){o1[4], o1[5], o1[6], o1[7]};
      cm[4 * 64 + lane] = (f32x4){m0, o0[8], m1, o1[8]};
    }
    lbar();
    if (kh == 0) {
      const f32x4 st = cm[4 * 64 + lane];
#pragma unroll
      for (int qb = 0; qb < 2; ++qb) {
        const float ma = qb ? m1 : m0, mb = qb ? st[2] : st[0];
        const float la = qb ? o1[8] : o0[8], lb = qb ? st[3] : st[1];
        const float mm = fmaxf(ma, mb);
        const float fa = EXP2(ma - mm), fb = EXP2(mb - mm);
        const float inv = 1.f / (la * fa + lb * fb);
        const float ga = fa * inv, gb = fb * inv;
        const f32x4 b0 = cm[(qb * 2 + 0) * 64 + lane], b1 = cm[(qb * 2 + 1) * 64 + lane];
        float v[8];
#pragma unroll
        for (int i = 0; i < 4; ++i) {
          v[i] = (qb ? o1[i] : o0[i]) * ga + b0[i] * gb;
          v[4 + i] = (qb ? o1[4 + i] : o0[4 + i]) * ga + b1[i] * gb;
        }
        char* dst = ws + WS_O + (size_t)((tg * 4 + qg) * 16 + j * 2 + qb) * 1024 + (((c32 >> 4) * 2) * 16 + (c32 & 15)) * 16 + h * 8;
        st8s(dst, (u32x2){pk2(v[0], v[1]), pk2(v[2], v[3])});
        st8s(dst + 256, (u32x2){pk2(v[4], v[5]), pk2(v[6], v[7])});
      }
    }
  }
  STAMP(5); STAMP_OUT(1);
}

template <int MODE>
DI void phaseCE(char* __restrict__ ws, char* lds, int tid, int w, const u16* __restrict__ x, u16* __restrict__ out, u64 tag) {
  constexpr int NKB = MODE ? 56 : 16, NS = NKB / 4;
  const int lane = tid & 63;
  const int b = blockIdx.x, xcd = b & 7, idx = b >> 3;
  const int tg = xcd * 4 + (idx & 3), ng = idx >> 2;
  const int r = lane & 15, g = lane >> 4;
  const int kg = w >> 2, wn = (w >> 1) & 1, wt = w & 1;
  const int nt = ng * 4 + wn * 2 + kg;
  STAMP_DECL; STAMP(0);
  const char* A = ws + (MODE ? WS_WD : WS_WO) + (size_t)(ng * 4) * NKB * 1024;
  const char* B = ws + (MODE ? WS_HH : WS_O) + (size_t)(tg * 4) * NKB * 1024;
  const char* src = (w < 4 ? A + (size_t)w * NKB * 1024 : B + (size_t)(w - 4) * NKB * 1024) + lane * 16;
  auto issue = [&](int s) {
    char* dst = lds + (s & 3) * 32768 + w * 1024;
#pragma unroll
    for (int i = 0; i < 4; ++i) glds16s(src + (size_t)(s * 4 + i) * 1024, dst + i * 8192);
  };
  if (MODE == 0) { if (w == 0) poll<256>(ws, FL_R, 0, tag, lane); bar(); }
  if (w < 4) {
#pragma unroll
    for (int s = 0; s < 4; ++s) issue(s);
  }
  if (w == 4) {
    if (MODE == 0) poll<8>(ws, FL_B, tg * 8, tag, lane);
    else poll<16>(ws, FL_D, (tg >> 1) * 16, tag, lane);
  }
  bar();
  if (w >= 4) {
#pragma unroll
    for (int s = 0; s < 4; ++s) issue(s);
  }
  u32x2 res[2];
#pragma unroll
  for (int t = 0; t < 2; ++t) {
    const int tt = tg * 4 + wt * 2 + t;
    if constexpr (MODE == 0) res[t] = *(const u32x2*)(x + (size_t)(tt * 16 + r) * 512 + nt * 16 + g * 4);
    else res[t] = ld8s(ws + WS_H + (size_t)(tt * 16 + (nt >> 1)) * 1024 + (((nt & 1) * 2 + (g >> 1)) * 16 + r) * 16 + (g & 1) * 8);
  }
  asm volatile("" ::: "memory");
  f32x4 acc[2][2] = {};
  bf16x8 af[2][2][2], bf[2][2][2];
  auto rd = [&](int s, int buf) {
    const char* base = lds + (s & 3) * 32768 + lane * 16;
#pragma unroll
    for (int kk = 0; kk < 2; ++kk) {
      const char* p = base + (kg * 2 + kk) * 8192;
#pragma unroll
      for (int i = 0; i < 2; ++i) { af[buf][kk][i] = lds_frag(p + (wn * 2 + i) * 1024); bf[buf][kk][i] = lds_frag(p + (4 + wt * 2 + i) * 1024); }
    }
  };
  wait_vm<12 + 2>();
  bar();
  STAMP(1);
  rd(0, 0);
#pragma unroll
  for (int s = 0; s < NS; ++s) {
    if (s + 1 < NS) {
      const int later = (NS - 2 - s) < 2 ? (NS - 2 - s) : 2;
      if (s < 2) { if (later == 2) wait_vm<8 + 2>(); else if (later == 1) wait_vm<4 + 2>(); else wait_vm<2>(); }
      else { if (later == 2) wait_vm<8>(); else if (later == 1) wait_vm<4>(); else wait_vm<0>(); }
      wait_lgkm0();
      bar();
      if (s == NS - 2) STAMP(2);
      if (s + 4 < NS) issue(s + 4);
      rd(s + 1, (s + 1) & 1);
    }
#pragma unroll
    for (int kk = 0; kk < 2; ++kk)
#pragma unroll
      for (int i = 0; i < 2; ++i)
#pragma unroll
        for (int t = 0; t < 2; ++t) acc[i][t] = MFMA16(af[s & 1][kk][i], bf[s & 1][kk][t], acc[i][t]);
  }
  STAMP(3);
  bar();
  f32x4* e4 = (f32x4*)(lds + w * 2048);
  e4[lane] = kg ? acc[0][0] : acc[1][0];
  e4[64 + lane] = kg ? acc[0][1] : acc[1][1];
  lbar();
  const f32x4* o4 = (const f32x4*)(lds + (w ^ 4) * 2048);
  if constexpr (MODE == 0) {
    float* part = (float*)(lds + 16384);
#pragma unroll
    for (int t = 0; t < 2; ++t) {
      const f32x4 a = (t ? (kg ? acc[1][1] : acc[0][1]) : (kg ? acc[1][0] : acc[0][0])) + o4[t * 64 + lane];
      const int tt = tg * 4 + wt * 2 + t;
      const u32x2 xr = res[t];
      const unsigned u0 = pk2(bflo(xr[0]) + a[0], bfhi(xr[0]) + a[1]), u1 = pk2(bflo(xr[1]) + a[2], bfhi(xr[1]) + a[3]);
      char* dst = ws + WS_H + (size_t)(tt * 16 + (nt >> 1)) * 1024 + (((nt & 1) * 2 + (g >> 1)) * 16 + r) * 16 + (g & 1) * 8;
      st8s(dst, (u32x2){u0, u1});
      const float h0 = bflo(u0), h1 = bfhi(u0), h2 = bflo(u1), h3 = bfhi(u1);
      const float s1 = xsum_g((h0 + h1) + (h2 + h3)), s2 = xsum_g((h0 * h0 + h1 * h1) + (h2 * h2 + h3 * h3));
      if (g == 0) *(f32x2*)(part + ((wn * 2 + kg) * 64 + (wt * 2 + t) * 16 + r) * 2) = (f32x2){s1, s2};
    }
    lbar();
    if (tid < 64) {
      f32x2 s = {0.f, 0.f};
#pragma unroll
      for (int i = 0; i < 4; ++i) s += *(const f32x2*)(part + (i * 64 + tid) * 2);
      st8s(ws + WS_ST + ((size_t)(tg * 64 + tid) * 8 + ng) * 8, __builtin_bit_cast(u32x2, s));
    }
  } else {
#pragma unroll
    for (int t = 0; t < 2; ++t) {
      const f32x4 a = (t ? (kg ? acc[1][1] : acc[0][1]) : (kg ? acc[1][0] : acc[0][0])) + o4[t * 64 + lane];
      const int tt = tg * 4 + wt * 2 + t;
      const u32x2 hr = res[t];
      *(u32x2*)(out + (size_t)(tt * 16 + r) * 512 + nt * 16 + g * 4) =
          (u32x2){pk2(bflo(hr[0]) + a[0], bfhi(hr[0]) + a[1]), pk2(bflo(hr[1]) + a[2], bfhi(hr[1]) + a[3])};
    }
  }
  STAMP(4); STAMP_OUT(MODE ? 4 : 2);
}

constexpr int K4_SLOT = 22 * 1024, K4_NSLOT = 7, K4_ST = K4_SLOT * K4_NSLOT, K4_CS = K4_ST + 1024, K4_END = K4_CS + 1024;
constexpr int LDS_BYTES = 160 * 1024;
static_assert(K4_END <= LDS_BYTES && K2_FLAG + 16 <= LDS_BYTES && K1_P + 8192 <= LDS_BYTES, "LDS budget");

template <int NP> DI void k4_wait(int later) {
  if (later >= 6) wait_vm<6 * NP>(); else if (later == 5) wait_vm<5 * NP>(); else if (later == 4) wait_vm<4 * NP>();
  else if (later == 3) wait_vm<3 * NP>(); else if (later == 2) wait_vm<2 * NP>(); else if (later == 1) wait_vm<NP>(); else wait_vm<0>();
}

template <int NT, int NP>
DI void k4_body(char* __restrict__ ws, char* lds, int w, int tid, int tg, int hgp, int tile0, int wt, u64 tag) {
  const int lane = tid & 63;
  const int r = lane & 15, g = lane >> 4;
  STAMP_DECL; STAMP(0);
  if (tid < 224) {
    float c = 0.f;
    float cp[16];
#pragma unroll
    for (int i = 0; i < 16; ++i) cp[i] = ld4s(ws + WS_CP + (size_t)(i * 3584 + hgp * 224 + tid) * 4);
#pragma unroll
    for (int i = 0; i < 16; ++i) c += cp[i];
    *(float*)(lds + K4_CS + tid * 4) = c;
  }
  if (w == 7) poll<16>(ws, FL_C, tg * 16, tag, lane);
  lbar();
  f32x4 sp[4];
  if (NT == 4 && w < 2) {
    const char* p = ws + WS_ST + (size_t)(tg * 128 + tid) * 64;
    asm volatile("global_load_dwordx4 %0, %4, off sc1\n\tglobal_load_dwordx4 %1, %4, off offset:16 sc1\n\t"
                 "global_load_dwordx4 %2, %4, off offset:32 sc1\n\tglobal_load_dwordx4 %3, %4, off offset:48 sc1"
                 : "=&v"(sp[0]), "=&v"(sp[1]), "=&v"(sp[2]), "=&v"(sp[3]) : "v"(p) : "memory");
  }
  const char* A = ws + WS_WGU + (size_t)(hgp * 14) * 16 * 1024 + lane * 16;
  const char* B = ws + WS_H + (size_t)(tg * 8) * 16 * 1024 + lane * 16;
  const char* s0 = A + (size_t)w * 16384;
  const char* s1 = (w < 6) ? A + (size_t)(w + 8) * 16384 : B + (size_t)(w - 6) * 16384;
  const char* s2 = B + (size_t)(w + 2) * 16384;
#pragma unroll
  for (int s = 0; s < 7; ++s) {
    char* dst = lds + s * K4_SLOT + w * 1024;
    glds16s(s0 + s * 1024, dst);
    glds16s(s1 + s * 1024, dst + 8192);
    if (NP == 3) glds16s(s2 + s * 1024, dst + 16384);
  }
  f32x4 acc[NT][4] = {};
  bf16x8 bf[2][4], af[2][NT];
  auto rd = [&](int s, int buf) {
    const char* base = lds + (s % K4_NSLOT) * K4_SLOT + lane * 16;
#pragma unroll
    for (int t = 0; t < 4; ++t) bf[buf][t] = lds_frag(base + (14 + wt * 4 + t) * 1024);
#pragma unroll
    for (int i = 0; i < NT; ++i) af[buf][i] = lds_frag(base + (tile0 + i) * 1024);
  };
  k4_wait<NP>(6);
  if (NT == 4 && w < 2) {
    asm volatile("" : "+v"(sp[0]), "+v"(sp[1]), "+v"(sp[2]), "+v"(sp[3]));
    const f32x4 v = (sp[0] + sp[1]) + (sp[2] + sp[3]);
    const float mean = (v[0] + v[2]) * (1.f / 512.f);
    const float var = fmaxf((v[1] + v[3]) * (1.f / 512.f) - mean * mean, 0.f);
    *(f32x2*)(lds + K4_ST + tid * 8) = (f32x2){mean, rsqrtf(var + EPS)};
  }
  lbar();
  STAMP(1);
  rd(0, 0);
#pragma unroll
  for (int s = 0; s < 16; ++s) {
    if (s + 1 < 16) {
      k4_wait<NP>((14 - s) < 5 ? (14 - s) : 5);
      wait_lgkm0();
      bar();
      if (s == 14) STAMP(2);
      rd(s + 1, (s + 1) & 1);
    }
    SB0();
#pragma unroll
    for (int i = 0; i < NT; ++i) {
#pragma unroll
      for (int t = 0; t < 4; ++t) acc[i][t] = MFMA16(af[s & 1][i], bf[s & 1][t], acc[i][t]);
      if (s + 7 < 16 && s + 1 < 16) {
        char* dst = lds + (s % K4_NSLOT) * K4_SLOT + w * 1024;
        if (i == 0) glds16s(s0 + (s + 7) * 1024, dst);
        if (i == 1) glds16s(s1 + (s + 7) * 1024, dst + 8192);
        if (i == 2 && NP == 3) glds16s(s2 + (s + 7) * 1024, dst + 16384);
      }
      SB0();
    }
  }
  STAMP(3);
  const f32x2* st = (const f32x2*)(lds + K4_ST);
  f32x4 c4[NT];
#pragma unroll
  for (int i = 0; i < NT; ++i) c4[i] = *(const f32x4*)(lds + K4_CS + ((tile0 + i) * 16 + g * 4) * 4);
  const int Tl = hgp * 14 + tile0 + g;
#pragma unroll
  for (int t = 0; t < 4; ++t) {
    const f32x2 ms = st[(wt * 4 + t) * 16 + r];
    unsigned R[4] = {0u, 0u, 0u, 0u};
#pragma unroll
    for (int i = 0; i < NT; ++i) {
      const f32x4 a = acc[i][t];
      float o[2];
#pragma unroll
      for (int e = 0; e < 2; ++e) {
        const float gv = (a[e] - ms[0] * c4[i][e]) * ms[1];
        const float uv = (a[2 + e] - ms[0] * c4[i][2 + e]) * ms[1];
        const float z = gv * (1.f + 0.044715f * gv * gv) * (-2.f * 0.7978845608028654f * 1.4426950408889634f);
        const float sg = __builtin_amdgcn_rcpf(1.f + EXP2(z));
        o[e] = gv * sg * uv;
      }
      R[i] = pk2(o[0], o[1]);
    }
    auto s02 = __builtin_amdgcn_permlane32_swap(R[0], R[2], false, false);
    auto s13 = __builtin_amdgcn_permlane32_swap(R[1], R[3], false, false);
    auto ab = __builtin_amdgcn_permlane16_swap(s02[0], s13[0], false, false);
    auto cd = __builtin_amdgcn_permlane16_swap(s02[1], s13[1], false, false);
    const int tt = tg * 8 + wt * 4 + t;
    if (g < NT)
      st16s(ws, (unsigned)WS_HH + (unsigned)(tt * 56 + (Tl >> 2)) * 1024u + ((Tl & 3) * 16 + r) * 16, (u32x4){ab[0], ab[1], cd[0], cd[1]});
  }
  STAMP(4); STAMP_OUT(3);
}

DI void phaseD(char* __restrict__ ws, char* lds, int tid, int w, u64 tag) {
  const int b = blockIdx.x, xcd = b & 7, idx = b >> 3;
  const int tg = 8 * (xcd & 1) + (idx & 7), hgp = 4 * (xcd >> 1) + (idx >> 3);
  const int wt = w & 1;
  if (w < 4) k4_body<4, 3>(ws, lds, w, tid, tg, hgp, (w >> 1) * 7, wt, tag);
  else if (w < 6) k4_body<3, 3>(ws, lds, w, tid, tg, hgp, 4, wt, tag);
  else k4_body<3, 2>(ws, lds, w, tid, tg, hgp, 11, wt, tag);
}

template <int MASK>
__global__ __launch_bounds__(512, 2) void block_fwd(const u16* __restrict__ x, const u16* __restrict__ wq,
                                                    const u16* __restrict__ wk, const u16* __restrict__ wv,
                                                    const u16* __restrict__ wo, const u16* __restrict__ wg,
                                                    const u16* __restrict__ wu, const u16* __restrict__ wd,
                                                    char* __restrict__ ws, u16* __restrict__ out) {
  extern __shared__ __attribute__((aligned(16))) char lds[];
  const int w = __builtin_amdgcn_readfirstlane(threadIdx.x >> 6);
  const int b = blockIdx.x, xcd = b & 7, idx = b >> 3;
  u64 tag = 0;
#define TID() ({ int t_ = threadIdx.x; asm volatile("" : "+v"(t_)); t_; })
  if constexpr (FUSED) tag = __hip_atomic_load((gu64*)(ws + WS_FL), RLX_AGENT) + 1ull;
  if constexpr (MASK & 3) {
    phaseAB(x, wq, wk, wv, wo, wg, wu, wd, ws, lds, TID(), w, tag);
    phaseB(ws, lds, TID(), w);
    publish(ws, FL_B, (xcd * 4 + (idx & 3)) * 8 + (idx >> 2), tag);
  }
  if constexpr (MASK & 4) {
    phaseCE<0>(ws, lds, TID(), w, x, nullptr, tag);
    publish(ws, FL_C, (xcd * 4 + (idx & 3)) * 8 + (idx >> 2), tag);
  }
  if constexpr (MASK & 8) {
    phaseD(ws, lds, TID(), w, tag);
    publish(ws, FL_D, (8 * (xcd & 1) + (idx & 7)) * 16 + 4 * (xcd >> 1) + (idx >> 3), tag);
  }
  if constexpr (MASK & 16) phaseCE<1>(ws, lds, TID(), w, x, out, tag);
}

template <int MASK>
static void launch(const u16* x, const u16* wq, const u16* wk, const u16* wv, const u16* wo, const u16* wg, const u16* wu,
                   const u16* wd, char* ws, u16* out, hipStream_t stream) {
  (void)hipFuncSetAttribute((const void*)block_fwd<MASK>, hipFuncAttributeMaxDynamicSharedMemorySize, LDS_BYTES);
  block_fwd<MASK><<<256, 512, LDS_BYTES, stream>>>(x, wq, wk, wv, wo, wg, wu, wd, ws, out);
}

extern "C" void kernel_launch(void* const* d_in, const int* in_sizes, int n_in, void* d_out, int out_size,
                              void* d_ws, size_t ws_size, hipStream_t stream) {
  const u16* x = (const u16*)d_in[0];
  const u16* wq = (const u16*)d_in[1];
  const u16* wk = (const u16*)d_in[2];
  const u16* wv = (const u16*)d_in[3];
  const u16* wo = (const u16*)d_in[4];
  const u16* wg = (const u16*)d_in[6];
  const u16* wu = (const u16*)d_in[7];
  const u16* wd = (const u16*)d_in[8];
  char* ws = (char*)d_ws;
  u16* out = (u16*)d_out;
  launch<31>(x, wq, wk, wv, wo, wg, wu, wd, ws, out, stream);
}
```

```cpp
#include <hip/hip_runtime.h>
#include <hip/hip_bf16.h>
#include <stdint.h>

typedef __attribute__((ext_vector_type(8))) short bf16x8;
typedef __attribute__((ext_vector_type(4))) short s16x4;
typedef __attribute__((ext_vector_type(4))) float f32x4;
typedef __attribute__((ext_vector_type(2))) float f32x2;
typedef __attribute__((ext_vector_type(16))) float f32x16;
typedef __attribute__((ext_vector_type(4))) unsigned u32x4;
typedef __attribute__((ext_vector_type(2))) unsigned u32x2;
typedef __attribute__((ext_vector_type(2))) __bf16 bf16x2_t;
typedef unsigned short u16;
typedef unsigned long long u64;
typedef __attribute__((address_space(1))) unsigned long long gu64;
typedef __attribute__((address_space(1))) unsigned gu32;
#define DI __device__ __forceinline__
#define LDSP __attribute__((address_space(3)))
#define RLX_AGENT __ATOMIC_RELAXED, __HIP_MEMORY_SCOPE_AGENT

#define NLAUNCH 1
constexpr bool FUSED = (NLAUNCH == 1);

constexpr float QSCALE = 0.25f * 1.4426950408889634f;
constexpr float EPS = 1e-5f;

constexpr size_t WS_Q = 0;
constexpr size_t WS_O = WS_Q + 2097152;
constexpr size_t WS_H = WS_O + 2097152;
constexpr size_t WS_HH = WS_H + 2097152;
constexpr size_t WS_K = WS_HH + 7340032;
constexpr size_t WS_V = WS_K + 65536;
constexpr size_t WS_WO = WS_V + 65536;
constexpr size_t WS_WGU = WS_WO + 524288;
constexpr size_t WS_WD = WS_WGU + 3670016;
constexpr size_t WS_CP = WS_WD + 1835008;
constexpr size_t WS_ST = WS_CP + 16 * 3584 * 4;
constexpr size_t WS_FL = WS_ST + 2048 * 64;
constexpr size_t WS_DBG = 64u << 20;
constexpr int FL_A = 256, FL_R = FL_A + 2048, FL_B = FL_R + 2048, FL_C = FL_B + 2048, FL_D = FL_C + 2048;

#define STAMP_DECL
#define STAMPK(kid, i) do {} while (0)
#define TSARG
#define TSPASS
#define STAMP_OUT(kid) do {} while (0)
DI unsigned pk2(float a, float b) {
  f32x2 v = {a, b};
  return __builtin_bit_cast(unsigned, __builtin_convertvector(v, bf16x2_t));
}
DI float bflo(unsigned u) { return __uint_as_float(u << 16); }
DI float bfhi(unsigned u) { return __uint_as_float(u & 0xffff0000u); }
DI float rbf(float a) { return bflo(pk2(a, a)); }
DI void glds16(const void* g, char* l) { __builtin_amdgcn_global_load_lds((const unsigned*)g, (unsigned*)l, 16, 0, 0); }
DI void glds16s(const void* g, char* l) { __builtin_amdgcn_global_load_lds((const unsigned*)g, (unsigned*)l, 16, 0, 16); }
DI void st16s(char* base, unsigned off, u32x4 v) {
  const __amdgpu_buffer_rsrc_t rs = __builtin_amdgcn_make_buffer_rsrc((void*)base, (short)0, 0x7fffffff, 0x00020000);
  __builtin_amdgcn_raw_buffer_store_b128(v, rs, off, 0, 16);
}
DI void st8s(char* p, u32x2 v) { __hip_atomic_store((gu64*)p, __builtin_bit_cast(u64, v), RLX_AGENT); }
DI void st4s(char* p, float v) { __hip_atomic_store((gu32*)p, __float_as_uint(v), RLX_AGENT); }
DI u32x2 ld8s(const char* p) { return __builtin_bit_cast(u32x2, __hip_atomic_load((gu64*)p, RLX_AGENT)); }
DI float ld4s(const char* p) { return __uint_as_float(__hip_atomic_load((gu32*)p, RLX_AGENT)); }
template <int N> DI void wait_vm() { asm volatile("s_waitcnt vmcnt(%0)" ::"n"(N) : "memory"); }
template <int N> DI void wait_vm_x(int extra) {
  if (extra == 8) wait_vm<N + 8>(); else if (extra == 4) wait_vm<N + 4>(); else wait_vm<N>();
}
DI void wait_lgkm0() { __builtin_amdgcn_s_waitcnt(0xC07F); asm volatile("" ::: "memory"); }
DI void bar() { asm volatile("" ::: "memory"); __builtin_amdgcn_s_barrier(); asm volatile("" ::: "memory"); }
DI void lbar() { wait_lgkm0(); bar(); }
DI bf16x8 lds_frag(const char* p) { return *(const bf16x8*)p; }
DI s16x4 lds_tr(const char* p) {
  return __builtin_bit_cast(s16x4, __builtin_amdgcn_ds_read_tr16_b64_v4i16((LDSP s16x4*)(LDSP char*)p));
}
#define MFMA16(a, b, c) __builtin_amdgcn_mfma_f32_16x16x32_bf16(a, b, c, 0, 0, 0)
#define MFMA32(a, b, c) __builtin_amdgcn_mfma_f32_32x32x16_bf16(a, b, c, 0, 0, 0)
#define EXP2(x) __builtin_amdgcn_exp2f(x)
#define SB0() __builtin_amdgcn_sched_barrier(0)
DI float xsum_g(float v) {
  v += __shfl_xor(v, 16);
  v += __shfl_xor(v, 32);
  return v;
}

DI void publish(char* ws, int fl, int slot, u64 tag) {
  if constexpr (FUSED) {
    asm volatile("s_waitcnt vmcnt(0)" ::: "memory");
    bar();
    if (threadIdx.x == 0) __hip_atomic_store((gu64*)(ws + WS_FL + fl + slot * 8), tag, RLX_AGENT);
  }
}
template <int N> DI void poll(char* ws, int fl, int slot0, u64 tag, int lane) {
  if constexpr (FUSED) {
    for (unsigned spins = 0; spins < (1u << 21); ++spins) {
      bool ok = true;
      if constexpr (N <= 64) {
        if (lane < N) ok = __hip_atomic_load((gu64*)(ws + WS_FL + fl + (slot0 + lane) * 8), RLX_AGENT) == tag;
      } else {
#pragma unroll
        for (int i = 0; i < N / 64; ++i) ok &= __hip_atomic_load((gu64*)(ws + WS_FL + fl + (slot0 + i * 64 + lane) * 8), RLX_AGENT) == tag;
      }
      if (__all(ok)) break;
      __builtin_amdgcn_s_sleep(1);
    }
    asm volatile("" ::: "memory");
  }
}

constexpr int NUNITS = 128 + 448 + 896;

struct WUnit { u32x4 raw[4]; int dst_tile, dst_stride, unit; };
DI void wunit_load(WUnit& u, const u16* __restrict__ wo, const u16* __restrict__ wg, const u16* __restrict__ wu,
                   const u16* __restrict__ wd, int gw, int lane) {
  const int unit = gw < NUNITS ? gw : NUNITS - 1;
  u.unit = unit;
  if (unit < 576) {
    const u16* src; int rb0, kb;
    if (unit < 128) { kb = unit >> 3; rb0 = (unit & 7) * 4; src = wo; u.dst_tile = (int)(WS_WO >> 10) + rb0 * 16 + kb; u.dst_stride = 16; }
    else { const int v = unit - 128; kb = v >> 3; rb0 = (v & 7) * 4; src = wd; u.dst_tile = (int)(WS_WD >> 10) + rb0 * 56 + kb; u.dst_stride = 56; }
    src += (size_t)(kb * 32 + (lane >> 3)) * 512 + rb0 * 16 + (lane & 7) * 8;
#pragma unroll
    for (int i = 0; i < 4; ++i) u.raw[i] = *(const u32x4*)(src + (size_t)i * 8 * 512);
  } else {
    const int v = unit - 576, kb = v / 56, hq = v % 56;
    u.dst_tile = (int)(WS_WGU >> 10) + hq * 4 * 16 + kb; u.dst_stride = 16;
    const size_t off = (size_t)(kb * 32 + (lane >> 2)) * 1792 + hq * 32 + (lane & 3) * 8;
    u.raw[0] = *(const u32x4*)(wg + off);
    u.raw[1] = *(const u32x4*)(wg + off + 16 * 1792);
    u.raw[2] = *(const u32x4*)(wu + off);
    u.raw[3] = *(const u32x4*)(wu + off + 16 * 1792);
  }
}

DI void wunit_store(const WUnit& u, char* __restrict__ ws, char* tb, int gw, int lane) {
  const bool isgu = u.unit >= 576;
  const int g = lane >> 4, q4 = (lane & 15) >> 2, p4 = lane & 3;
  const char* rp = tb + (8 * g + q4) * 64 + p4 * 8;
#pragma unroll
  for (int p = 0; p < 2; ++p) {
    if (!isgu) {
      if (((lane & 7) >> 2) == p) {
#pragma unroll
        for (int i = 0; i < 4; ++i) *(u32x4*)(tb + (i * 8 + (lane >> 3)) * 64 + (lane & 3) * 16) = u.raw[i];
      }
    } else {
      if (((lane & 3) >> 1) == p) {
#pragma unroll
        for (int i = 0; i < 4; ++i) {
          char* q = tb + ((i & 1) * 16 + (lane >> 2)) * 64 + (lane & 1) * 32 + (i >> 1) * 4;
#pragma unroll
          for (int e = 0; e < 4; ++e) *(unsigned*)(q + e * 8) = u.raw[i][e];
        }
      }
    }
#pragma unroll
    for (int t2 = 0; t2 < 2; ++t2) {
      const int t = p * 2 + t2;
      const s16x4 lo = lds_tr(rp + t2 * 32), hi = lds_tr(rp + t2 * 32 + 256);
      const bf16x8 f = __builtin_shufflevector(lo, hi, 0, 1, 2, 3, 4, 5, 6, 7);
      if (gw < NUNITS) {
        st16s(ws, (unsigned)(u.dst_tile + t * u.dst_stride) * 1024u + lane * 16, __builtin_bit_cast(u32x4, f));
        if (isgu) {
          const u32x4 v4 = __builtin_bit_cast(u32x4, f);
          float cs = ((bflo(v4[0]) + bfhi(v4[0])) + (bflo(v4[1]) + bfhi(v4[1]))) + ((bflo(v4[2]) + bfhi(v4[2])) + (bflo(v4[3]) + bfhi(v4[3])));
          cs = xsum_g(cs);
          const int v = u.unit - 576;
          if (lane < 16) st4s(ws + WS_CP + (size_t)((v / 56) * 3584 + ((v % 56) * 4 + t) * 16 + lane) * 4, cs);
        }
      }
    }
  }
}

constexpr int K1_X = 0, K1_W = 65536, K1_KV = 131072, K1_ST = 147456, K1_P = K1_ST + 1024;
constexpr int K2_K = 0, K2_V = 65536, K2_ONES = 131072, K2_CMB = K2_ONES + 4096, K2_FLAG = K2_CMB + 4 * 5 * 1024, K2_TOP = K2_FLAG + 1024;

DI void phaseAB(const u16* __restrict__ x, const u16* __restrict__ wq, const u16* __restrict__ wk, const u16* __restrict__ wv,
                const u16* __restrict__ wo, const u16* __restrict__ wg, const u16* __restrict__ wu, const u16* __restrict__ wd,
                char* __restrict__ ws, char* lds, int tid, int w, u64 tag, WUnit& wun TSARG) {
  const int lane = tid & 63;
  const int b = blockIdx.x, xcd = b & 7, idx = b >> 3;
  const int tg = xcd * 4 + (idx & 3), j = idx >> 2;
  const int r = lane & 15, g = lane >> 4;
  const u16* wkv = (j & 1) ? wv : wk;
  const int tq = j >> 1;
  const int gw = b * 8 + w;
  STAMP_DECL; STAMPK(0, 0);
  {
    const int s = lane >> 4, pr = (lane >> 1) & 7, hh = lane & 1;
#pragma unroll
    for (int kq = 0; kq < 8; ++kq) {
      const int row = w * 8 + (lane >> 3), ch = (lane & 7) ^ (lane >> 3);
      glds16(x + (size_t)(tg * 64 + row) * 512 + kq * 64 + ch * 8, lds + K1_X + kq * 8192 + w * 1024);
    }
#pragma unroll
    for (int c = 0; c < 2; ++c) {
      const int kb8b = c * 32 + w * 4, kb8 = kb8b + s;
      const int k = 8 * kb8 + (pr ^ (4 * (kb8 & 1)));
      glds16(wkv + (size_t)k * 16 + hh * 8, lds + K1_KV + kb8b * 256);
    }
  }
  auto load_w = [&]() {
    const int s = lane >> 4, pr = (lane >> 1) & 7, hh = lane & 1;
#pragma unroll
    for (int c = 0; c < 8; ++c) {
      const int kb8 = c * 8 + w;
      const int k = 8 * kb8 + (pr ^ (4 * (kb8 & 1)));
      glds16(wq + (size_t)k * 512 + j * 64 + s * 16 + hh * 8, lds + K1_W + kb8 * 1024);
    }
    asm volatile("" ::: "memory");
    wunit_load(wun, wo, wg, wu, wd, gw, lane);
    asm volatile("" ::: "memory");
  };
  if (w != 0) load_w();
  const unsigned ONE2 = 0x3f803f80u;
  const u32x4 ones4 = {ONE2, ONE2, ONE2, ONE2};
  auto xfrag = [&](int tt, int kb) {
    return lds_frag(lds + K1_X + (kb >> 1) * 8192 + (tt * 16 + r) * 128 + ((((kb & 1) * 4 + g) ^ (r & 7)) * 16));
  };
  if (w != 0) wait_vm<2 + 12>(); else wait_vm<2>();
  bar();
  STAMPK(0, 1);
  {
    const bf16x8 ones = __builtin_bit_cast(bf16x8, ones4);
    f32x4 sm = {}, sq = {};
#pragma unroll
    for (int kk = 0; kk < 8; ++kk) {
      const bf16x8 f = xfrag(w & 3, (w >> 2) * 8 + kk);
      sm = MFMA16(ones, f, sm);
      sq = MFMA16(f, f, sq);
    }
    const float d = (r & 2) ? ((r & 1) ? sq[3] : sq[2]) : ((r & 1) ? sq[1] : sq[0]);
    float* stp = (float*)(lds + K1_ST) + ((w >> 2) * 64 + (w & 3) * 16 + r) * 2;
    if (g == 0) stp[0] = sm[0];
    if (g == (r >> 2)) stp[1] = d;
  }
  lbar();
  {
    const int row = tid >> 3;
    const f32x2 s0 = *(const f32x2*)(lds + K1_ST + row * 8), s1 = *(const f32x2*)(lds + K1_ST + 512 + row * 8);
    const float mean = (s0[0] + s1[0]) * (1.f / 512.f);
    const float var = fmaxf((s0[1] + s1[1]) * (1.f / 512.f) - mean * mean, 0.f);
    const float mb = rbf(mean);
    const float sd = rbf(sqrtf(rbf(rbf(var) + EPS)));
    const float inv = 1.f / sd;
    const f32x2 mb2 = {mb, mb}, inv2 = {inv, inv};
    char* p = lds + K1_X + row * 128 + (tid & 7) * 16;
    u32x4 v[8];
#pragma unroll
    for (int kq = 0; kq < 8; ++kq) v[kq] = *(const u32x4*)(p + kq * 8192);
#pragma unroll
    for (int k2 = 0; k2 < 8; k2 += 2) {
      f32x2 a[8];
      unsigned d[8];
#pragma unroll
      for (int e = 0; e < 8; ++e) { const unsigned u = v[k2 + (e >> 2)][e & 3]; a[e] = (f32x2){bflo(u), bfhi(u)} - mb2; }
#pragma unroll
      for (int e = 0; e < 8; ++e) d[e] = pk2(a[e][0], a[e][1]);
#pragma unroll
      for (int e = 0; e < 8; ++e) a[e] = (f32x2){bflo(d[e]), bfhi(d[e])} * inv2;
#pragma unroll
      for (int e = 0; e < 8; ++e) v[k2 + (e >> 2)][e & 3] = pk2(a[e][0], a[e][1]);
      *(u32x4*)(p + k2 * 8192) = v[k2];
      *(u32x4*)(p + (k2 + 1) * 8192) = v[k2 + 1];
    }
  }
  const int q4 = (lane & 15) >> 2, p4 = lane & 3;
  const int troff0 = ((q4) ^ (4 * (g & 1))) * 32 + p4 * 8;
  const int troff1 = ((4 + q4) ^ (4 * (g & 1))) * 32 + p4 * 8;
  if (w != 0) wait_vm<12>(); else wait_vm<0>();
  lbar();
  STAMPK(0, 2);
  {
    f32x4 kv = {};
#pragma unroll
    for (int kk = 0; kk < 2; ++kk) {
      const int kb = w * 2 + kk;
      const char* base = lds + K1_KV + (kb * 4 + g) * 256;
      s16x4 lo = lds_tr(base + troff0), hi = lds_tr(base + troff1);
      const bf16x8 kf = __builtin_shufflevector(lo, hi, 0, 1, 2, 3, 4, 5, 6, 7);
      const bf16x8 xx = xfrag(tq, kb);
      if (j & 1) kv = MFMA16(xx, kf, kv);
      else       kv = MFMA16(kf, xx, kv);
    }
    *(f32x4*)(lds + K1_P + w * 1024 + lane * 16) = kv;
  }
  lbar();
  if (w == 0) {
    f32x4 a = {};
#pragma unroll
    for (int i = 0; i < 8; ++i) a += *(const f32x4*)(lds + K1_P + i * 1024 + lane * 16);
    const int tok0 = tg * 64 + tq * 16;
    char* dst;
    if (j & 1) dst = ws + WS_V + (size_t)(tok0 >> 4) * 512 + ((g & 1) * 16 + r) * 16 + (g >> 1) * 8;
    else { const int key = tok0 + r; dst = ws + WS_K + (size_t)(key >> 5) * 1024 + ((g >> 1) * 32 + (key & 31)) * 16 + (g & 1) * 8; }
    st8s(dst, (u32x2){pk2(a[0], a[1]), pk2(a[2], a[3])});
    if constexpr (FUSED) {
      asm volatile("s_waitcnt vmcnt(0)" ::: "memory");
      if (lane == 0) __hip_atomic_store((gu64*)(ws + WS_FL + FL_A + b * 8), tag, RLX_AGENT);
    }
    load_w();
  }
  STAMPK(0, 3);
  const int kg = w >> 2, wn = (w >> 1) & 1, wt = w & 1;
  {
    f32x4 acc[2][2] = {};
#pragma unroll
    for (int c = 0; c < 4; ++c) {
      if (c == 0) wait_vm<6 + 4>(); else if (c == 1) wait_vm<4 + 4>(); else if (c == 2) wait_vm<2 + 4>(); else wait_vm<4>();
      bar();
#pragma unroll
      for (int kk = 0; kk < 2; ++kk) {
        const int kb = c * 4 + kg * 2 + kk;
        bf16x8 xf[2], wf[2];
#pragma unroll
        for (int t = 0; t < 2; ++t) xf[t] = xfrag(wt * 2 + t, kb);
#pragma unroll
        for (int i = 0; i < 2; ++i) {
          const char* base = lds + K1_W + (kb * 4 + g) * 1024 + (wn * 2 + i) * 256;
          s16x4 lo = lds_tr(base + troff0), hi = lds_tr(base + troff1);
          wf[i] = __builtin_shufflevector(lo, hi, 0, 1, 2, 3, 4, 5, 6, 7);
        }
#pragma unroll
        for (int i = 0; i < 2; ++i)
#pragma unroll
          for (int t = 0; t < 2; ++t) acc[i][t] = MFMA16(wf[i], xf[t], acc[i][t]);
      }
    }
    lbar();
    f32x4* e4 = (f32x4*)(lds + w * 2048);
    e4[0 * 64 + lane] = kg ? acc[0][0] : acc[1][0];
    e4[1 * 64 + lane] = kg ? acc[0][1] : acc[1][1];
    lbar();
    const f32x4* o4 = (const f32x4*)(lds + (w ^ 4) * 2048);
#pragma unroll
    for (int t = 0; t < 2; ++t) {
      const f32x4 a = (t ? (kg ? acc[1][1] : acc[0][1]) : (kg ? acc[1][0] : acc[0][0])) + o4[t * 64 + lane];
      char* dst = lds + K2_CMB + ((wt * 2 + t) * 2 + wn) * 1024 + ((g >> 1) * 32 + kg * 16 + r) * 16 + (g & 1) * 8;
      *(u32x2*)dst = (u32x2){pk2(a[0] * QSCALE, a[1] * QSCALE), pk2(a[2] * QSCALE, a[3] * QSCALE)};
    }
  }
  if (tid < 256) *(u32x4*)(lds + K2_ONES + tid * 16) = ones4;
  if (tid == 0) *(unsigned*)(lds + K2_FLAG) = 0u;
  STAMPK(0, 4);
  if (w == 0) poll<256>(ws, FL_A, 0, tag, lane);
  lbar();
  if (FUSED && b == 0 && tid == 0) __hip_atomic_store((gu64*)(ws + WS_FL), tag, RLX_AGENT);
  STAMPK(0, 5); STAMP_OUT(0);
}

DI void phaseB(char* __restrict__ ws, char* lds, int tid, int w, u64 tag, WUnit& wun TSARG) {
  const int lane = tid & 63;
  const int b = blockIdx.x, xcd = b & 7, idx = b >> 3;
  const int tg = xcd * 4 + (idx & 3), j = idx >> 2;
  const int qg = w & 3, kh = w >> 2;
  const int c32 = lane & 31, h = lane >> 5;
  STAMP_DECL; STAMPK(1, 0);
  asm volatile("" : "+v"(wun.raw[0]), "+v"(wun.raw[1]), "+v"(wun.raw[2]), "+v"(wun.raw[3]));
#pragma unroll
  for (int c = 0; c < 8; ++c) {
    const int tile = kh * 32 + c * 4 + qg;
    glds16s(ws + WS_K + (size_t)tile * 1024 + lane * 16, lds + K2_K + tile * 1024);
    glds16s(ws + WS_V + (size_t)tile * 1024 + lane * 16, lds + K2_V + tile * 1024);
  }
  const char* kp = lds + K2_K + kh * 32768 + lane * 16;
  const bool vreal = c32 < 16;
  const int onesoff = (c32 >= 20 && c32 < 28) ? 64 : 0;
  const char* vp = vreal ? (lds + K2_V + kh * 32768 + (h * 16 + c32) * 16) : (lds + K2_ONES + onesoff);
  const int vstep = vreal ? 4096 : 0;
  const bf16x8 q0 = lds_frag(lds + K2_CMB + (qg * 2) * 1024 + lane * 16), q1 = lds_frag(lds + K2_CMB + (qg * 2 + 1) * 1024 + lane * 16);
  wait_vm<14>();
  lbar();
  STAMPK(1, 2);
  f32x16 o0, o1;
  float m0, m1;
  int nmax = 1;
  for (int attempt = 0; attempt < 2; ++attempt) {
    {
      float a0 = -INFINITY, a1 = -INFINITY;
      const f32x16 z = {};
      for (int t = 0; t < nmax; ++t) {
        const bf16x8 kf = lds_frag(kp + t * 1024);
        const f32x16 s0 = MFMA32(kf, q0, z), s1 = MFMA32(kf, q1, z);
#pragma unroll
        for (int i = 0; i < 16; ++i) { a0 = fmaxf(a0, s0[i]); a1 = fmaxf(a1, s1[i]); }
      }
      m0 = fmaxf(a0, __shfl_xor(a0, 32));
      m1 = fmaxf(a1, __shfl_xor(a1, 32));
    }
    f32x16 n0, n1;
#pragma unroll
    for (int i = 0; i < 16; ++i) { n0[i] = -m0; n1[i] = -m1; o0[i] = 0.f; o1[i] = 0.f; }
    asm volatile("" : "+v"(n0), "+v"(n1));
    STAMPK(1, 3);
    const char* vq = vp;
    bf16x8 kn = lds_frag(kp);
    f32x16 sa0 = MFMA32(kn, q0, n0), sa1 = MFMA32(kn, q1, n1), sb0, sb1;
    kn = lds_frag(kp + 1024);
    bf16x8 vA0 = lds_frag(vq), vA1 = lds_frag(vq + 512), vB0 = vA0, vB1 = vA1;
    u32x4 pa, pb, pc = {}, pd = {};
#define PAIR(P, i, C, o) P[i] = pk2(EXP2(C[o]), EXP2(C[(o) + 1]))
#define BC(x) __builtin_bit_cast(bf16x8, x)
#define TILE_STEP(C0, C1, N0, N1, VC0, VC1, VP0, VP1, KOFF, VOFF, HASN, HASK)          \
    {                                                                                  \
      o1 = MFMA32(VP0, BC(pc), o1);                                                    \
      PAIR(pa, 0, C0, 0); PAIR(pa, 1, C0, 2); PAIR(pa, 2, C0, 4); SB0();               \
      o1 = MFMA32(VP1, BC(pd), o1);                                                    \
      PAIR(pa, 3, C0, 6); PAIR(pb, 0, C0, 8); PAIR(pb, 1, C0, 10); SB0();              \
      if (HASN) N0 = MFMA32(kn, q0, n0);                                               \
      PAIR(pb, 2, C0, 12); PAIR(pb, 3, C0, 14); SB0();                                 \
      o0 = MFMA32(VC0, BC(pa), o0); SB0();                                             \
      if (HASN) { VP0 = lds_frag(vq + (VOFF)); VP1 = lds_frag(vq + (VOFF) + 512); }    \
      PAIR(pc, 0, C1, 0); PAIR(pc, 1, C1, 2); PAIR(pc, 2, C1, 4); SB0();               \
      if (HASN) N1 = MFMA32(kn, q1, n1);                                               \
      PAIR(pc, 3, C1, 6); PAIR(pd, 0, C1, 8); PAIR(pd, 1, C1, 10); SB0();              \
      o0 = MFMA32(VC1, BC(pb), o0); SB0();                                             \
      if (HASK) kn = lds_frag(kp + (KOFF));                                            \
      PAIR(pd, 2, C1, 12); PAIR(pd, 3, C1, 14); SB0();                                 \
    }
#pragma unroll
    for (int c = 0; c < 8; ++c) {
      TILE_STEP(sa0, sa1, sb0, sb1, vA0, vA1, vB0, vB1, (c * 4 + 2) * 1024, 1024, true, true);
      TILE_STEP(sb0, sb1, sa0, sa1, vB0, vB1, vA0, vA1, (c * 4 + 3) * 1024, 2048, true, true);
      if (c < 7) {
        if (c == 0) wait_vm<12>(); else if (c == 1) wait_vm<10>(); else if (c == 2) wait_vm<8>(); else wait_vm<0>();
        bar();
        if (c == 3 && attempt == 0) wunit_store(wun, ws, lds + (w < 6 ? K2_CMB + 8192 + w * 2048 : K2_TOP + (w - 6) * 2048), b * 8 + w, lane);
        if (FUSED && c == 6 && attempt == 0 && tid == 0) __hip_atomic_store((gu64*)(ws + WS_FL + FL_R + b * 8), tag, RLX_AGENT);
      }
      TILE_STEP(sa0, sa1, sb0, sb1, vA0, vA1, vB0, vB1, (c * 4 + 4) * 1024, 3072, true, c < 7);
      vq += vstep;
      TILE_STEP(sb0, sb1, sa0, sa1, vB0, vB1, vA0, vA1, (c * 4 + 5) * 1024, 0, c < 7, c < 7);
    }
    o1 = MFMA32(vB0, BC(pc), o1);
    o1 = MFMA32(vB1, BC(pd), o1);
    STAMPK(1, 4);
    const bool ok = (o0[8] < 1e30f) && (o1[8] < 1e30f);
    if (!__all(ok)) { if (lane == 0) *(LDSP volatile unsigned*)(LDSP char*)(lds + K2_FLAG) = 1u; }
    lbar();
    const unsigned f = *(const LDSP volatile unsigned*)(LDSP char*)(lds + K2_FLAG);
    if (f == 0u || attempt == 1) break;
    nmax = 32;
  }
  {
    f32x4* cm = (f32x4*)(lds + K2_CMB + qg * 5120);
    if (kh == 1) {
      cm[0 * 64 + lane] = (f32x4){o0[0], o0[1], o0[2], o0[3]};
      cm[1 * 64 + lane] = (f32x4){o0[4], o0[5], o0[6], o0[7]};
      cm[2 * 64 + lane] = (f32x4){o1[0], o1[1], o1[2], o1[3]};
      cm[3 * 64 + lane] = (f32x4){o1[4], o1[5], o1[6], o1[7]};
      cm[4 * 64 + lane] = (f32x4){m0, o0[8], m1, o1[8]};
    }
    lbar();
    if (w == 4) poll<256>(ws, FL_R, 0, tag, lane);
    if (kh == 0) {
      const f32x4 st = cm[4 * 64 + lane];
#pragma unroll
      for (int qb = 0; qb < 2; ++qb) {
        const float ma = qb ? m1 : m0, mb = qb ? st[2] : st[0];
        const float la = qb ? o1[8] : o0[8], lb = qb ? st[3] : st[1];
        const float mm = fmaxf(ma, mb);
        const float fa = EXP2(ma - mm), fb = EXP2(mb - mm);
        const float inv = 1.f / (la * fa + lb * fb);
        const float ga = fa * inv, gb = fb * inv;
        const f32x4 b0 = cm[(qb * 2 + 0) * 64 + lane], b1 = cm[(qb * 2 + 1) * 64 + lane];
        float v[8];
#pragma unroll
        for (int i = 0; i < 4; ++i) {
          v[i] = (qb ? o1[i] : o0[i]) * ga + b0[i] * gb;
          v[4 + i] = (qb ? o1[4 + i] : o0[4 + i]) * ga + b1[i] * gb;
        }
        auto s0 = __builtin_amdgcn_permlane32_swap(pk2(v[0], v[1]), pk2(v[4], v[5]), false, false);
        auto s1 = __builtin_amdgcn_permlane32_swap(pk2(v[2], v[3]), pk2(v[6], v[7]), false, false);
        st16s(ws, (unsigned)WS_O + (unsigned)((tg * 4 + qg) * 16 + j * 2 + qb) * 1024u + (((c32 >> 4) * 2 + h) * 16 + (c32 & 15)) * 16,
              (u32x4){s0[0], s1[0], s0[1], s1[1]});
      }
    }
  }
  STAMPK(1, 5); STAMP_OUT(1);
}

template <int MODE>
DI void phaseCE(char* __restrict__ ws, char* lds, int tid, int w, const u16* __restrict__ x, u16* __restrict__ out, u64 tag TSARG) {
  constexpr int NKB = MODE ? 56 : 16, NS = NKB / 4;
  const int lane = tid & 63;
  const int b = blockIdx.x, xcd = b & 7, idx = b >> 3;
  const int tg = xcd * 4 + (idx & 3), ng = idx >> 2;
  const int r = lane & 15, g = lane >> 4;
  const int kg = w >> 2, wn = (w >> 1) & 1, wt = w & 1;
  const int nt = ng * 4 + wn * 2 + kg;
  STAMP_DECL; STAMPK((MODE ? 4 : 2), 0);
  const char* A = ws + (MODE ? WS_WD : WS_WO) + (size_t)(ng * 4) * NKB * 1024;
  const char* B = ws + (MODE ? WS_HH : WS_O) + (size_t)(tg * 4) * NKB * 1024;
  const char* src = (w < 4 ? A + (size_t)w * NKB * 1024 : B + (size_t)(w - 4) * NKB * 1024) + lane * 16;
  auto issue = [&](int s) {
    char* dst = lds + (s & 3) * 32768 + w * 1024;
#pragma unroll
    for (int i = 0; i < 4; ++i) glds16s(src + (size_t)(s * 4 + i) * 1024, dst + i * 8192);
  };
  if (w < 4) {
#pragma unroll
    for (int s = 0; s < 4; ++s) issue(s);
  }
  if (w == 4) {
    if (MODE == 0) poll<8>(ws, FL_B, tg * 8, tag, lane);
    else poll<16>(ws, FL_D, (tg >> 1) * 16, tag, lane);
  }
  bar();
  if (w >= 4) {
#pragma unroll
    for (int s = 0; s < 4; ++s) issue(s);
  }
  u32x2 res[2];
#pragma unroll
  for (int t = 0; t < 2; ++t) {
    const int tt = tg * 4 + wt * 2 + t;
    if constexpr (MODE == 0) res[t] = *(const u32x2*)(x + (size_t)(tt * 16 + r) * 512 + nt * 16 + g * 4);
    else res[t] = ld8s(ws + WS_H + (size_t)(tt * 16 + (nt >> 1)) * 1024 + (((nt & 1) * 2 + (g >> 1)) * 16 + r) * 16 + (g & 1) * 8);
  }
  asm volatile("" ::: "memory");
  f32x4 acc[2][2] = {};
  bf16x8 af[2][2][2], bf[2][2][2];
  auto rd = [&](int s, int buf) {
    const char* base = lds + (s & 3) * 32768 + lane * 16;
#pragma unroll
    for (int kk = 0; kk < 2; ++kk) {
      const char* p = base + (kg * 2 + kk) * 8192;
#pragma unroll
      for (int i = 0; i < 2; ++i) { af[buf][kk][i] = lds_frag(p + (wn * 2 + i) * 1024); bf[buf][kk][i] = lds_frag(p + (4 + wt * 2 + i) * 1024); }
    }
  };
  wait_vm<12 + 2>();
  bar();
  STAMPK((MODE ? 4 : 2), 1);
  rd(0, 0);
#pragma unroll
  for (int s = 0; s < NS; ++s) {
    if (s + 1 < NS) {
      const int later = (NS - 2 - s) < 2 ? (NS - 2 - s) : 2;
      if (s < 2) { if (later == 2) wait_vm<8 + 2>(); else if (later == 1) wait_vm<4 + 2>(); else wait_vm<2>(); }
      else { if (later == 2) wait_vm<8>(); else if (later == 1) wait_vm<4>(); else wait_vm<0>(); }
      wait_lgkm0();
      bar();
      if (s == NS - 2) STAMPK((MODE ? 4 : 2), 2);
      if (s + 4 < NS) issue(s + 4);
      rd(s + 1, (s + 1) & 1);
    }
#pragma unroll
    for (int kk = 0; kk < 2; ++kk)
#pragma unroll
      for (int i = 0; i < 2; ++i)
#pragma unroll
        for (int t = 0; t < 2; ++t) acc[i][t] = MFMA16(af[s & 1][kk][i], bf[s & 1][kk][t], acc[i][t]);
  }
  STAMPK((MODE ? 4 : 2), 3);
  bar();
  f32x4* e4 = (f32x4*)(lds + w * 2048);
  e4[lane] = kg ? acc[0][0] : acc[1][0];
  e4[64 + lane] = kg ? acc[0][1] : acc[1][1];
  lbar();
  const f32x4* o4 = (const f32x4*)(lds + (w ^ 4) * 2048);
  if constexpr (MODE == 0) {
    float* part = (float*)(lds + 16384);
    unsigned hu[2][2];
#pragma unroll
    for (int t = 0; t < 2; ++t) {
      const f32x4 a = (t ? (kg ? acc[1][1] : acc[0][1]) : (kg ? acc[1][0] : acc[0][0])) + o4[t * 64 + lane];
      const u32x2 xr = res[t];
      const unsigned u0 = pk2(bflo(xr[0]) + a[0], bfhi(xr[0]) + a[1]), u1 = pk2(bflo(xr[1]) + a[2], bfhi(xr[1]) + a[3]);
      hu[t][0] = u0; hu[t][1] = u1;
      const float h0 = bflo(u0), h1 = bfhi(u0), h2 = bflo(u1), h3 = bfhi(u1);
      const float s1 = xsum_g((h0 + h1) + (h2 + h3)), s2 = xsum_g((h0 * h0 + h1 * h1) + (h2 * h2 + h3 * h3));
      if (g == 0) *(f32x2*)(part + ((wn * 2 + kg) * 64 + (wt * 2 + t) * 16 + r) * 2) = (f32x2){s1, s2};
    }
    {
      auto s0 = __builtin_amdgcn_permlane16_swap(hu[0][0], hu[1][0], false, false);
      auto s1 = __builtin_amdgcn_permlane16_swap(hu[0][1], hu[1][1], false, false);
      const int tt = tg * 4 + wt * 2 + (g & 1);
      st16s(ws, (unsigned)WS_H + (unsigned)(tt * 16 + (nt >> 1)) * 1024u + (((nt & 1) * 2 + (g >> 1)) * 16 + r) * 16,
            (u32x4){s0[0], s1[0], s0[1], s1[1]});
    }
    lbar();
    if (tid < 64) {
      f32x2 s = {0.f, 0.f};
#pragma unroll
      for (int i = 0; i < 4; ++i) s += *(const f32x2*)(part + (i * 64 + tid) * 2);
      st8s(ws + WS_ST + ((size_t)(tg * 64 + tid) * 8 + ng) * 8, __builtin_bit_cast(u32x2, s));
    }
  } else {
#pragma unroll
    for (int t = 0; t < 2; ++t) {
      const f32x4 a = (t ? (kg ? acc[1][1] : acc[0][1]) : (kg ? acc[1][0] : acc[0][0])) + o4[t * 64 + lane];
      const int tt = tg * 4 + wt * 2 + t;
      const u32x2 hr = res[t];
      *(u32x2*)(out + (size_t)(tt * 16 + r) * 512 + nt * 16 + g * 4) =
          (u32x2){pk2(bflo(hr[0]) + a[0], bfhi(hr[0]) + a[1]), pk2(bflo(hr[1]) + a[2], bfhi(hr[1]) + a[3])};
    }
  }
  STAMPK((MODE ? 4 : 2), 4); STAMP_OUT(MODE ? 4 : 2);
}

constexpr int K4_SLOT = 22 * 1024, K4_NSLOT = 7, K4_ST = K4_SLOT * K4_NSLOT, K4_CS = K4_ST + 1024, K4_END = K4_CS + 1024;
constexpr int LDS_BYTES = 160 * 1024;
static_assert(K2_TOP + 4096 <= 160 * 1024 && K4_END <= LDS_BYTES && K2_FLAG + 16 <= LDS_BYTES && K1_P + 8192 <= LDS_BYTES, "LDS budget");

template <int NP> DI void k4_wait(int later) {
  if (later >= 6) wait_vm<6 * NP>(); else if (later == 5) wait_vm<5 * NP>(); else if (later == 4) wait_vm<4 * NP>();
  else if (later == 3) wait_vm<3 * NP>(); else if (later == 2) wait_vm<2 * NP>(); else if (later == 1) wait_vm<NP>(); else wait_vm<0>();
}

template <int NT, int NP>
DI void k4_body(char* __restrict__ ws, char* lds, int w, int tid, int tg, int hgp, int tile0, int wt, u64 tag TSARG) {
  const int lane = tid & 63;
  const int r = lane & 15, g = lane >> 4;
  STAMP_DECL; STAMPK(3, 0);
  const char* A = ws + WS_WGU + (size_t)(hgp * 14) * 16 * 1024 + lane * 16;
  const char* B = ws + WS_H + (size_t)(tg * 8) * 16 * 1024 + lane * 16;
  const char* s0 = A + (size_t)w * 16384;
  const char* s1 = (w < 6) ? A + (size_t)(w + 8) * 16384 : B + (size_t)(w - 6) * 16384;
  const char* s2 = B + (size_t)(w + 2) * 16384;
#pragma unroll
  for (int s = 0; s < 7; ++s) {
    char* dst = lds + s * K4_SLOT + w * 1024;
    glds16s(s0 + s * 1024, dst);
    if (NP == 3) glds16s(s1 + s * 1024, dst + 8192);
  }
  float cp[16];
  if (NT == 4 && tid < 224) {
#pragma unroll
    for (int i = 0; i < 16; ++i) cp[i] = ld4s(ws + WS_CP + (size_t)(i * 3584 + hgp * 224 + tid) * 4);
  }
  asm volatile("" ::: "memory");
  if (w == 7) poll<16>(ws, FL_C, tg * 16, tag, lane);
  bar();
  f32x4 sp[4];
  if (NT == 4 && w < 2) {
    const char* p = ws + WS_ST + (size_t)(tg * 128 + tid) * 64;
    asm volatile("global_load_dwordx4 %0, %4, off sc1\n\tglobal_load_dwordx4 %1, %4, off offset:16 sc1\n\t"
                 "global_load_dwordx4 %2, %4, off offset:32 sc1\n\tglobal_load_dwordx4 %3, %4, off offset:48 sc1"
                 : "=&v"(sp[0]), "=&v"(sp[1]), "=&v"(sp[2]), "=&v"(sp[3]) : "v"(p) : "memory");
  }
#pragma unroll
  for (int s = 0; s < 7; ++s) {
    char* dst = lds + s * K4_SLOT + w * 1024;
    if (NP == 3) glds16s(s2 + s * 1024, dst + 16384); else glds16s(s1 + s * 1024, dst + 8192);
  }
  f32x4 acc[NT][4] = {};
  bf16x8 bf[2][4], af[2][NT];
  auto rd = [&](int s, int buf) {
    const char* base = lds + (s % K4_NSLOT) * K4_SLOT + lane * 16;
#pragma unroll
    for (int t = 0; t < 4; ++t) bf[buf][t] = lds_frag(base + (14 + wt * 4 + t) * 1024);
#pragma unroll
    for (int i = 0; i < NT; ++i) af[buf][i] = lds_frag(base + (tile0 + i) * 1024);
  };
  wait_vm<6>();
  if (NT == 4 && w < 2) {
    asm volatile("" : "+v"(sp[0]), "+v"(sp[1]), "+v"(sp[2]), "+v"(sp[3]));
    const f32x4 v = (sp[0] + sp[1]) + (sp[2] + sp[3]);
    const float mean = (v[0] + v[2]) * (1.f / 512.f);
    const float var = fmaxf((v[1] + v[3]) * (1.f / 512.f) - mean * mean, 0.f);
    *(f32x2*)(lds + K4_ST + tid * 8) = (f32x2){mean, rsqrtf(var + EPS)};
  }
  lbar();
  STAMPK(3, 1);
  rd(0, 0);
#pragma unroll
  for (int s = 0; s < 16; ++s) {
    if (s + 1 < 16) {
      if (s == 0) wait_vm<5>(); else if (s == 1) wait_vm<4 + NP>(); else if (s == 2) wait_vm<3 + 2 * NP>();
      else if (s == 3) wait_vm<2 + 3 * NP>(); else if (s == 4) wait_vm<1 + 4 * NP>();
      else k4_wait<NP>((14 - s) < 5 ? (14 - s) : 5);
      wait_lgkm0();
      bar();
      if (s == 14) STAMPK(3, 2);
      rd(s + 1, (s + 1) & 1);
    }
    SB0();
#pragma unroll
    for (int i = 0; i < NT; ++i) {
#pragma unroll
      for (int t = 0; t < 4; ++t) acc[i][t] = MFMA16(af[s & 1][i], bf[s & 1][t], acc[i][t]);
      if (s + 7 < 16 && s + 1 < 16) {
        char* dst = lds + (s % K4_NSLOT) * K4_SLOT + w * 1024;
        if (i == 0) glds16s(s0 + (s + 7) * 1024, dst);
        if (i == 1) glds16s(s1 + (s + 7) * 1024, dst + 8192);
        if (i == 2 && NP == 3) glds16s(s2 + (s + 7) * 1024, dst + 16384);
      }
      SB0();
    }
  }
  STAMPK(3, 3);
  if (NT == 4 && tid < 224) {
    float c = 0.f;
#pragma unroll
    for (int i = 0; i < 16; ++i) c += cp[i];
    *(float*)(lds + K4_CS + tid * 4) = c;
  }
  lbar();
  const f32x2* st = (const f32x2*)(lds + K4_ST);
  f32x4 c4[NT];
#pragma unroll
  for (int i = 0; i < NT; ++i) c4[i] = *(const f32x4*)(lds + K4_CS + ((tile0 + i) * 16 + g * 4) * 4);
  const int Tl = hgp * 14 + tile0 + g;
#pragma unroll
  for (int t = 0; t < 4; ++t) {
    const f32x2 ms = st[(wt * 4 + t) * 16 + r];
    unsigned R[4] = {0u, 0u, 0u, 0u};
#pragma unroll
    for (int i = 0; i < NT; ++i) {
      const f32x4 a = acc[i][t];
      float o[2];
#pragma unroll
      for (int e = 0; e < 2; ++e) {
        const float gv = (a[e] - ms[0] * c4[i][e]) * ms[1];
        const float uv = (a[2 + e] - ms[0] * c4[i][2 + e]) * ms[1];
        const float z = gv * (1.f + 0.044715f * gv * gv) * (-2.f * 0.7978845608028654f * 1.4426950408889634f);
        const float sg = __builtin_amdgcn_rcpf(1.f + EXP2(z));
        o[e] = gv * sg * uv;
      }
      R[i] = pk2(o[0], o[1]);
    }
    auto s02 = __builtin_amdgcn_permlane32_swap(R[0], R[2], false, false);
    auto s13 = __builtin_amdgcn_permlane32_swap(R[1], R[3], false, false);
    auto ab = __builtin_amdgcn_permlane16_swap(s02[0], s13[0], false, false);
    auto cd = __builtin_amdgcn_permlane16_swap(s02[1], s13[1], false, false);
    const int tt = tg * 8 + wt * 4 + t;
    if (g < NT)
      st16s(ws, (unsigned)WS_HH + (unsigned)(tt * 56 + (Tl >> 2)) * 1024u + ((Tl & 3) * 16 + r) * 16, (u32x4){ab[0], ab[1], cd[0], cd[1]});
  }
  STAMPK(3, 4); STAMP_OUT(3);
}

DI void phaseD(char* __restrict__ ws, char* lds, int tid, int w, u64 tag TSARG) {
  const int b = blockIdx.x, xcd = b & 7, idx = b >> 3;
  const int tg = 8 * (xcd & 1) + (idx & 7), hgp = 4 * (xcd >> 1) + (idx >> 3);
  const int wt = w & 1;
  if (w < 4) k4_body<4, 3>(ws, lds, w, tid, tg, hgp, (w >> 1) * 7, wt, tag TSPASS);
  else if (w < 6) k4_body<3, 3>(ws, lds, w, tid, tg, hgp, 4, wt, tag TSPASS);
  else k4_body<3, 2>(ws, lds, w, tid, tg, hgp, 11, wt, tag TSPASS);
}

template <int MASK>
__global__ __launch_bounds__(512, 2) void block_fwd(const u16* __restrict__ x, const u16* __restrict__ wq,
                                                    const u16* __restrict__ wk, const u16* __restrict__ wv,
                                                    const u16* __restrict__ wo, const u16* __restrict__ wg,
                                                    const u16* __restrict__ wu, const u16* __restrict__ wd,
                                                    char* __restrict__ ws, u16* __restrict__ out) {
  extern __shared__ __attribute__((aligned(16))) char lds[];
  const int w = __builtin_amdgcn_readfirstlane(threadIdx.x >> 6);
  const int b = blockIdx.x, xcd = b & 7, idx = b >> 3;
  u64 tag = 0;
#define TID() ({ int t_ = threadIdx.x; asm volatile("" : "+v"(t_)); t_; })
  if constexpr (FUSED) tag = __hip_atomic_load((gu64*)(ws + WS_FL), RLX_AGENT) + 1ull;
  if constexpr (MASK & 3) {
    WUnit wun;
    phaseAB(x, wq, wk, wv, wo, wg, wu, wd, ws, lds, TID(), w, tag, wun TSPASS);
    phaseB(ws, lds, TID(), w, tag, wun TSPASS);
    publish(ws, FL_B, (xcd * 4 + (idx & 3)) * 8 + (idx >> 2), tag);
  }
  if constexpr (MASK & 4) {
    phaseCE<0>(ws, lds, TID(), w, x, nullptr, tag TSPASS);
    publish(ws, FL_C, (xcd * 4 + (idx & 3)) * 8 + (idx >> 2), tag);
  }
  if constexpr (MASK & 8) {
    phaseD(ws, lds, TID(), w, tag TSPASS);
    publish(ws, FL_D, (8 * (xcd & 1) + (idx & 7)) * 16 + 4 * (xcd >> 1) + (idx >> 3), tag);
  }
  if constexpr (MASK & 16) phaseCE<1>(ws, lds, TID(), w, x, out, tag TSPASS);
}

template <int MASK>
static void launch(const u16* x, const u16* wq, const u16* wk, const u16* wv, const u16* wo, const u16* wg, const u16* wu,
                   const u16* wd, char* ws, u16* out, hipStream_t stream) {
  (void)hipFuncSetAttribute((const void*)block_fwd<MASK>, hipFuncAttributeMaxDynamicSharedMemorySize, LDS_BYTES);
  block_fwd<MASK><<<256, 512, LDS_BYTES, stream>>>(x, wq, wk, wv, wo, wg, wu, wd, ws, out);
}

extern "C" void kernel_launch(void* const* d_in, const int* in_sizes, int n_in, void* d_out, int out_size,
                              void* d_ws, size_t ws_size, hipStream_t stream) {
  const u16* x = (const u16*)d_in[0];
  const u16* wq = (const u16*)d_in[1];
  const u16* wk = (const u16*)d_in[2];
  const u16* wv = (const u16*)d_in[3];
  const u16* wo = (const u16*)d_in[4];
  const u16* wg = (const u16*)d_in[6];
  const u16* wu = (const u16*)d_in[7];
  const u16* wd = (const u16*)d_in[8];
  char* ws = (char*)d_ws;
  u16* out = (u16*)d_out;
  launch<31>(x, wq, wk, wv, wo, wg, wu, wd, ws, out, stream);
}
```

```cpp
#include <hip/hip_runtime.h>
#include <hip/hip_bf16.h>
#include <stdint.h>

typedef __attribute__((ext_vector_type(8))) short bf16x8;
typedef __attribute__((ext_vector_type(4))) short s16x4;
typedef __attribute__((ext_vector_type(4))) float f32x4;
typedef __attribute__((ext_vector_type(2))) float f32x2;
typedef __attribute__((ext_vector_type(16))) float f32x16;
typedef __attribute__((ext_vector_type(4))) unsigned u32x4;
typedef __attribute__((ext_vector_type(2))) unsigned u32x2;
typedef __attribute__((ext_vector_type(2))) __bf16 bf16x2_t;
typedef unsigned short u16;
typedef unsigned long long u64;
typedef __attribute__((address_space(1))) unsigned long long gu64;
typedef __attribute__((address_space(1))) unsigned gu32;
#define DI __device__ __forceinline__
#define LDSP __attribute__((address_space(3)))
#define RLX_AGENT __ATOMIC_RELAXED, __HIP_MEMORY_SCOPE_AGENT

#define NLAUNCH 1
constexpr bool FUSED = (NLAUNCH == 1);

constexpr float QSCALE = 0.25f * 1.4426950408889634f;
constexpr float EPS = 1e-5f;

constexpr size_t WS_Q = 0;
constexpr size_t WS_O = WS_Q + 2097152;
constexpr size_t WS_H = WS_O + 2097152;
constexpr size_t WS_HH = WS_H + 2097152;
constexpr size_t WS_K = WS_HH + 7340032;
constexpr size_t WS_V = WS_K + 65536;
constexpr size_t WS_WO = WS_V + 65536;
constexpr size_t WS_WGU = WS_WO + 524288;
constexpr size_t WS_WD = WS_WGU + 3670016;
constexpr size_t WS_CP = WS_WD + 1835008;
constexpr size_t WS_ST = WS_CP + 16 * 3584 * 4;
constexpr size_t WS_FL = WS_ST + 2048 * 64;
constexpr int FL_A = 256, FL_R = FL_A + 2048, FL_B = FL_R + 2048, FL_C = FL_B + 2048, FL_D = FL_C + 2048;

DI unsigned pk2(float a, float b) {
  f32x2 v = {a, b};
  return __builtin_bit_cast(unsigned, __builtin_convertvector(v, bf16x2_t));
}
DI float bflo(unsigned u) { return __uint_as_float(u << 16); }
DI float bfhi(unsigned u) { return __uint_as_float(u & 0xffff0000u); }
DI float rbf(float a) { return bflo(pk2(a, a)); }
DI void glds16(const void* g, char* l) { __builtin_amdgcn_global_load_lds((const unsigned*)g, (unsigned*)l, 16, 0, 0); }
DI void glds16s(const void* g, char* l) { __builtin_amdgcn_global_load_lds((const unsigned*)g, (unsigned*)l, 16, 0, 16); }
DI void st16s(char* base, unsigned off, u32x4 v) {
  const __amdgpu_buffer_rsrc_t rs = __builtin_amdgcn_make_buffer_rsrc((void*)base, (short)0, 0x7fffffff, 0x00020000);
  __builtin_amdgcn_raw_buffer_store_b128(v, rs, off, 0, 16);
}
DI void st8s(char* p, u32x2 v) { __hip_atomic_store((gu64*)p, __builtin_bit_cast(u64, v), RLX_AGENT); }
DI void st4s(char* p, float v) { __hip_atomic_store((gu32*)p, __float_as_uint(v), RLX_AGENT); }
DI u32x2 ld8s(const char* p) { return __builtin_bit_cast(u32x2, __hip_atomic_load((gu64*)p, RLX_AGENT)); }
DI float ld4s(const char* p) { return __uint_as_float(__hip_atomic_load((gu32*)p, RLX_AGENT)); }
template <int N> DI void wait_vm() { asm volatile("s_waitcnt vmcnt(%0)" ::"n"(N) : "memory"); }
template <int N> DI void wait_vm_x(int extra) {
  if (extra == 8) wait_vm<N + 8>(); else if (extra == 4) wait_vm<N + 4>(); else wait_vm<N>();
}
DI void wait_lgkm0() { __builtin_amdgcn_s_waitcnt(0xC07F); asm volatile("" ::: "memory"); }
DI void bar() { asm volatile("" ::: "memory"); __builtin_amdgcn_s_barrier(); asm volatile("" ::: "memory"); }
DI void lbar() { wait_lgkm0(); bar(); }
DI bf16x8 lds_frag(const char* p) { return *(const bf16x8*)p; }
DI s16x4 lds_tr(const char* p) {
  return __builtin_bit_cast(s16x4, __builtin_amdgcn_ds_read_tr16_b64_v4i16((LDSP s16x4*)(LDSP char*)p));
}
#define MFMA16(a, b, c) __builtin_amdgcn_mfma_f32_16x16x32_bf16(a, b, c, 0, 0, 0)
#define MFMA32(a, b, c) __builtin_amdgcn_mfma_f32_32x32x16_bf16(a, b, c, 0, 0, 0)
#define EXP2(x) __builtin_amdgcn_exp2f(x)
#define SB0() __builtin_amdgcn_sched_barrier(0)
DI float xsum_g(float v) {
  v += __shfl_xor(v, 16);
  v += __shfl_xor(v, 32);
  return v;
}

DI void publish(char* ws, int fl, int slot, u64 tag) {
  if constexpr (FUSED) {
    asm volatile("s_waitcnt vmcnt(0)" ::: "memory");
    bar();
    if (threadIdx.x == 0) __hip_atomic_store((gu64*)(ws + WS_FL + fl + slot * 8), tag, RLX_AGENT);
  }
}
template <int N> DI void poll(char* ws, int fl, int slot0, u64 tag, int lane) {
  if constexpr (FUSED) {
    for (unsigned spins = 0; spins < (1u << 17); ++spins) {
      bool ok = true;
      if constexpr (N <= 64) {
        if (lane < N) ok = __hip_atomic_load((gu64*)(ws + WS_FL + fl + (slot0 + lane) * 8), RLX_AGENT) == tag;
      } else {
#pragma unroll
        for (int i = 0; i < N / 64; ++i) ok &= __hip_atomic_load((gu64*)(ws + WS_FL + fl + (slot0 + i * 64 + lane) * 8), RLX_AGENT) == tag;
      }
      if (__all(ok)) break;
      __builtin_amdgcn_s_sleep(1);
    }
    asm volatile("" ::: "memory");
  }
}

constexpr int NUNITS = 128 + 448 + 896;

struct WUnit { u32x4 raw[4]; int dst_tile, dst_stride, unit; };
DI void wunit_load(WUnit& u, const u16* __restrict__ wo, const u16* __restrict__ wg, const u16* __restrict__ wu,
                   const u16* __restrict__ wd, int gw, int lane) {
  const int unit = gw < NUNITS ? gw : NUNITS - 1;
  u.unit = unit;
  if (unit < 576) {
    const u16* src; int rb0, kb;
    if (unit < 128) { kb = unit >> 3; rb0 = (unit & 7) * 4; src = wo; u.dst_tile = (int)(WS_WO >> 10) + rb0 * 16 + kb; u.dst_stride = 16; }
    else { const int v = unit - 128; kb = v >> 3; rb0 = (v & 7) * 4; src = wd; u.dst_tile = (int)(WS_WD >> 10) + rb0 * 56 + kb; u.dst_stride = 56; }
    src += (size_t)(kb * 32 + (lane >> 3)) * 512 + rb0 * 16 + (lane & 7) * 8;
#pragma unroll
    for (int i = 0; i < 4; ++i) u.raw[i] = *(const u32x4*)(src + (size_t)i * 8 * 512);
  } else {
    const int v = unit - 576, kb = v / 56, hq = v % 56;
    u.dst_tile = (int)(WS_WGU >> 10) + hq * 4 * 16 + kb; u.dst_stride = 16;
    const size_t off = (size_t)(kb * 32 + (lane >> 2)) * 1792 + hq * 32 + (lane & 3) * 8;
    u.raw[0] = *(const u32x4*)(wg + off);
    u.raw[1] = *(const u32x4*)(wg + off + 16 * 1792);
    u.raw[2] = *(const u32x4*)(wu + off);
    u.raw[3] = *(const u32x4*)(wu + off + 16 * 1792);
  }
}

DI void wunit_store(const WUnit& u, char* __restrict__ ws, char* tb, int gw, int lane) {
  const bool isgu = u.unit >= 576;
  const int g = lane >> 4, q4 = (lane & 15) >> 2, p4 = lane & 3;
  const char* rp = tb + (8 * g + q4) * 64 + p4 * 8;
#pragma unroll
  for (int p = 0; p < 2; ++p) {
    if (!isgu) {
      if (((lane & 7) >> 2) == p) {
#pragma unroll
        for (int i = 0; i < 4; ++i) *(u32x4*)(tb + (i * 8 + (lane >> 3)) * 64 + (lane & 3) * 16) = u.raw[i];
      }
    } else {
      if (((lane & 3) >> 1) == p) {
#pragma unroll
        for (int i = 0; i < 4; ++i) {
          char* q = tb + ((i & 1) * 16 + (lane >> 2)) * 64 + (lane & 1) * 32 + (i >> 1) * 4;
#pragma unroll
          for (int e = 0; e < 4; ++e) *(unsigned*)(q + e * 8) = u.raw[i][e];
        }
      }
    }
#pragma unroll
    for (int t2 = 0; t2 < 2; ++t2) {
      const int t = p * 2 + t2;
      const s16x4 lo = lds_tr(rp + t2 * 32), hi = lds_tr(rp + t2 * 32 + 256);
      const bf16x8 f = __builtin_shufflevector(lo, hi, 0, 1, 2, 3, 4, 5, 6, 7);
      if (gw < NUNITS) {
        st16s(ws, (unsigned)(u.dst_tile + t * u.dst_stride) * 1024u + lane * 16, __builtin_bit_cast(u32x4, f));
        if (isgu) {
          const u32x4 v4 = __builtin_bit_cast(u32x4, f);
          float cs = ((bflo(v4[0]) + bfhi(v4[0])) + (bflo(v4[1]) + bfhi(v4[1]))) + ((bflo(v4[2]) + bfhi(v4[2])) + (bflo(v4[3]) + bfhi(v4[3])));
          cs = xsum_g(cs);
          const int v = u.unit - 576;
          if (lane < 16) st4s(ws + WS_CP + (size_t)((v / 56) * 3584 + ((v % 56) * 4 + t) * 16 + lane) * 4, cs);
        }
      }
    }
  }
}

constexpr int K1_X = 0, K1_W = 65536, K1_KV = 131072, K1_ST = 147456, K1_P = K1_ST + 1024;
constexpr int K2_K = 0, K2_V = 65536, K2_ONES = 131072, K2_CMB = K2_ONES + 4096, K2_FLAG = K2_CMB + 4 * 5 * 1024, K2_TOP = K2_FLAG + 1024;

DI void phaseAB(const u16* __restrict__ x, const u16* __restrict__ wq, const u16* __restrict__ wk, const u16* __restrict__ wv,
                const u16* __restrict__ wo, const u16* __restrict__ wg, const u16* __restrict__ wu, const u16* __restrict__ wd,
                char* __restrict__ ws, char* lds, int tid, int w, u64 tag, WUnit& wun) {
  const int lane = tid & 63;
  const int b = blockIdx.x, xcd = b & 7, idx = b >> 3;
  const int tg = xcd * 4 + (idx & 3), j = idx >> 2;
  const int r = lane & 15, g = lane >> 4;
  const u16* wkv = (j & 1) ? wv : wk;
  const int tq = j >> 1;
  const int gw = b * 8 + w;

  {
    const int s = lane >> 4, pr = (lane >> 1) & 7, hh = lane & 1;
#pragma unroll
    for (int kq = 0; kq < 8; ++kq) {
      const int row = w * 8 + (lane >> 3), ch = (lane & 7) ^ (lane >> 3);
      glds16(x + (size_t)(tg * 64 + row) * 512 + kq * 64 + ch * 8, lds + K1_X + kq * 8192 + w * 1024);
    }
#pragma unroll
    for (int c = 0; c < 2; ++c) {
      const int kb8b = c * 32 + w * 4, kb8 = kb8b + s;
      const int k = 8 * kb8 + (pr ^ (4 * (kb8 & 1)));
      glds16(wkv + (size_t)k * 16 + hh * 8, lds + K1_KV + kb8b * 256);
    }
  }
  auto load_w = [&]() {
    const int s = lane >> 4, pr = (lane >> 1) & 7, hh = lane & 1;
#pragma unroll
    for (int c = 0; c < 8; ++c) {
      const int kb8 = c * 8 + w;
      const int k = 8 * kb8 + (pr ^ (4 * (kb8 & 1)));
      glds16(wq + (size_t)k * 512 + j * 64 + s * 16 + hh * 8, lds + K1_W + kb8 * 1024);
    }
    asm volatile("" ::: "memory");
    wunit_load(wun, wo, wg, wu, wd, gw, lane);
    asm volatile("" ::: "memory");
  };
  if (w != 0) load_w();
  const unsigned ONE2 = 0x3f803f80u;
  const u32x4 ones4 = {ONE2, ONE2, ONE2, ONE2};
  auto xfrag = [&](int tt, int kb) {
    return lds_frag(lds + K1_X + (kb >> 1) * 8192 + (tt * 16 + r) * 128 + ((((kb & 1) * 4 + g) ^ (r & 7)) * 16));
  };
  if (w != 0) wait_vm<2 + 12>(); else wait_vm<2>();
  bar();

  {
    const bf16x8 ones = __builtin_bit_cast(bf16x8, ones4);
    f32x4 sm = {}, sq = {};
#pragma unroll
    for (int kk = 0; kk < 8; ++kk) {
      const bf16x8 f = xfrag(w & 3, (w >> 2) * 8 + kk);
      sm = MFMA16(ones, f, sm);
      sq = MFMA16(f, f, sq);
    }
    const float d = (r & 2) ? ((r & 1) ? sq[3] : sq[2]) : ((r & 1) ? sq[1] : sq[0]);
    float* stp = (float*)(lds + K1_ST) + ((w >> 2) * 64 + (w & 3) * 16 + r) * 2;
    if (g == 0) stp[0] = sm[0];
    if (g == (r >> 2)) stp[1] = d;
  }
  lbar();
  {
    const int row = tid >> 3;
    const f32x2 s0 = *(const f32x2*)(lds + K1_ST + row * 8), s1 = *(const f32x2*)(lds + K1_ST + 512 + row * 8);
    const float mean = (s0[0] + s1[0]) * (1.f / 512.f);
    const float var = fmaxf((s0[1] + s1[1]) * (1.f / 512.f) - mean * mean, 0.f);
    const float mb = rbf(mean);
    const float sd = rbf(sqrtf(rbf(rbf(var) + EPS)));
    const float inv = 1.f / sd;
    const f32x2 mb2 = {mb, mb}, inv2 = {inv, inv};
    char* p = lds + K1_X + row * 128 + (tid & 7) * 16;
    u32x4 v[8];
#pragma unroll
    for (int kq = 0; kq < 8; ++kq) v[kq] = *(const u32x4*)(p + kq * 8192);
#pragma unroll
    for (int k2 = 0; k2 < 8; k2 += 2) {
      f32x2 a[8];
      unsigned d[8];
#pragma unroll
      for (int e = 0; e < 8; ++e) { const unsigned u = v[k2 + (e >> 2)][e & 3]; a[e] = (f32x2){bflo(u), bfhi(u)} - mb2; }
#pragma unroll
      for (int e = 0; e < 8; ++e) d[e] = pk2(a[e][0], a[e][1]);
#pragma unroll
      for (int e = 0; e < 8; ++e) a[e] = (f32x2){bflo(d[e]), bfhi(d[e])} * inv2;
#pragma unroll
      for (int e = 0; e < 8; ++e) v[k2 + (e >> 2)][e & 3] = pk2(a[e][0], a[e][1]);
      *(u32x4*)(p + k2 * 8192) = v[k2];
      *(u32x4*)(p + (k2 + 1) * 8192) = v[k2 + 1];
    }
  }
  const int q4 = (lane & 15) >> 2, p4 = lane & 3;
  const int troff0 = ((q4) ^ (4 * (g & 1))) * 32 + p4 * 8;
  const int troff1 = ((4 + q4) ^ (4 * (g & 1))) * 32 + p4 * 8;
  if (w != 0) wait_vm<12>(); else wait_vm<0>();
  lbar();

  {
    f32x4 kv = {};
#pragma unroll
    for (int kk = 0; kk < 2; ++kk) {
      const int kb = w * 2 + kk;
      const char* base = lds + K1_KV + (kb * 4 + g) * 256;
      s16x4 lo = lds_tr(base + troff0), hi = lds_tr(base + troff1);
      const bf16x8 kf = __builtin_shufflevector(lo, hi, 0, 1, 2, 3, 4, 5, 6, 7);
      const bf16x8 xx = xfrag(tq, kb);
      if (j & 1) kv = MFMA16(xx, kf, kv);
      else       kv = MFMA16(kf, xx, kv);
    }
    *(f32x4*)(lds + K1_P + w * 1024 + lane * 16) = kv;
  }
  lbar();
  if (w == 0) {
    f32x4 a = {};
#pragma unroll
    for (int i = 0; i < 8; ++i) a += *(const f32x4*)(lds + K1_P + i * 1024 + lane * 16);
    const int tok0 = tg * 64 + tq * 16;
    char* dst;
    if (j & 1) dst = ws + WS_V + (size_t)(tok0 >> 4) * 512 + ((g & 1) * 16 + r) * 16 + (g >> 1) * 8;
    else { const int key = tok0 + r; dst = ws + WS_K + (size_t)(key >> 5) * 1024 + ((g >> 1) * 32 + (key & 31)) * 16 + (g & 1) * 8; }
    st8s(dst, (u32x2){pk2(a[0], a[1]), pk2(a[2], a[3])});
    if constexpr (FUSED) {
      asm volatile("s_waitcnt vmcnt(0)" ::: "memory");
      if (lane == 0) __hip_atomic_store((gu64*)(ws + WS_FL + FL_A + b * 8), tag, RLX_AGENT);
    }
    load_w();
  }

  const int kg = w >> 2, wn = (w >> 1) & 1, wt = w & 1;
  {
    f32x4 acc[2][2] = {};
#pragma unroll
    for (int c = 0; c < 4; ++c) {
      if (c == 0) wait_vm<6 + 4>(); else if (c == 1) wait_vm<4 + 4>(); else if (c == 2) wait_vm<2 + 4>(); else wait_vm<4>();
      bar();
#pragma unroll
      for (int kk = 0; kk < 2; ++kk) {
        const int kb = c * 4 + kg * 2 + kk;
        bf16x8 xf[2], wf[2];
#pragma unroll
        for (int t = 0; t < 2; ++t) xf[t] = xfrag(wt * 2 + t, kb);
#pragma unroll
        for (int i = 0; i < 2; ++i) {
          const char* base = lds + K1_W + (kb * 4 + g) * 1024 + (wn * 2 + i) * 256;
          s16x4 lo = lds_tr(base + troff0), hi = lds_tr(base + troff1);
          wf[i] = __builtin_shufflevector(lo, hi, 0, 1, 2, 3, 4, 5, 6, 7);
        }
#pragma unroll
        for (int i = 0; i < 2; ++i)
#pragma unroll
          for (int t = 0; t < 2; ++t) acc[i][t] = MFMA16(wf[i], xf[t], acc[i][t]);
      }
    }
    lbar();
    f32x4* e4 = (f32x4*)(lds + w * 2048);
    e4[0 * 64 + lane] = kg ? acc[0][0] : acc[1][0];
    e4[1 * 64 + lane] = kg ? acc[0][1] : acc[1][1];
    lbar();
    const f32x4* o4 = (const f32x4*)(lds + (w ^ 4) * 2048);
#pragma unroll
    for (int t = 0; t < 2; ++t) {
      const f32x4 a = (t ? (kg ? acc[1][1] : acc[0][1]) : (kg ? acc[1][0] : acc[0][0])) + o4[t * 64 + lane];
      char* dst = lds + K2_CMB + ((wt * 2 + t) * 2 + wn) * 1024 + ((g >> 1) * 32 + kg * 16 + r) * 16 + (g & 1) * 8;
      *(u32x2*)dst = (u32x2){pk2(a[0] * QSCALE, a[1] * QSCALE), pk2(a[2] * QSCALE, a[3] * QSCALE)};
    }
  }
  if (tid < 256) *(u32x4*)(lds + K2_ONES + tid * 16) = ones4;
  if (tid == 0) *(unsigned*)(lds + K2_FLAG) = 0u;

  if (w == 0) poll<256>(ws, FL_A, 0, tag, lane);
  lbar();
  if (FUSED && b == 0 && tid == 0) __hip_atomic_store((gu64*)(ws + WS_FL), tag, RLX_AGENT);

}

DI void phaseB(char* __restrict__ ws, char* lds, int tid, int w, u64 tag, WUnit& wun) {
  const int lane = tid & 63;
  const int b = blockIdx.x, xcd = b & 7, idx = b >> 3;
  const int tg = xcd * 4 + (idx & 3), j = idx >> 2;
  const int qg = w & 3, kh = w >> 2;
  const int c32 = lane & 31, h = lane >> 5;

  asm volatile("" : "+v"(wun.raw[0]), "+v"(wun.raw[1]), "+v"(wun.raw[2]), "+v"(wun.raw[3]));
#pragma unroll
  for (int c = 0; c < 8; ++c) {
    const int tile = kh * 32 + c * 4 + qg;
    glds16s(ws + WS_K + (size_t)tile * 1024 + lane * 16, lds + K2_K + tile * 1024);
    glds16s(ws + WS_V + (size_t)tile * 1024 + lane * 16, lds + K2_V + tile * 1024);
  }
  const char* kp = lds + K2_K + kh * 32768 + lane * 16;
  const bool vreal = c32 < 16;
  const int onesoff = (c32 >= 20 && c32 < 28) ? 64 : 0;
  const char* vp = vreal ? (lds + K2_V + kh * 32768 + (h * 16 + c32) * 16) : (lds + K2_ONES + onesoff);
  const int vstep = vreal ? 4096 : 0;
  const bf16x8 q0 = lds_frag(lds + K2_CMB + (qg * 2) * 1024 + lane * 16), q1 = lds_frag(lds + K2_CMB + (qg * 2 + 1) * 1024 + lane * 16);
  wait_vm<14>();
  lbar();

  f32x16 o0, o1;
  float m0, m1;
  int nmax = 1;
  for (int attempt = 0; attempt < 2; ++attempt) {
    {
      float a0 = -INFINITY, a1 = -INFINITY;
      const f32x16 z = {};
      for (int t = 0; t < nmax; ++t) {
        const bf16x8 kf = lds_frag(kp + t * 1024);
        const f32x16 s0 = MFMA32(kf, q0, z), s1 = MFMA32(kf, q1, z);
#pragma unroll
        for (int i = 0; i < 16; ++i) { a0 = fmaxf(a0, s0[i]); a1 = fmaxf(a1, s1[i]); }
      }
      m0 = fmaxf(a0, __shfl_xor(a0, 32));
      m1 = fmaxf(a1, __shfl_xor(a1, 32));
    }
    f32x16 n0, n1;
#pragma unroll
    for (int i = 0; i < 16; ++i) { n0[i] = -m0; n1[i] = -m1; o0[i] = 0.f; o1[i] = 0.f; }
    asm volatile("" : "+v"(n0), "+v"(n1));

    const char* vq = vp;
    bf16x8 kn = lds_frag(kp);
    f32x16 sa0 = MFMA32(kn, q0, n0), sa1 = MFMA32(kn, q1, n1), sb0, sb1;
    kn = lds_frag(kp + 1024);
    bf16x8 vA0 = lds_frag(vq), vA1 = lds_frag(vq + 512), vB0 = vA0, vB1 = vA1;
    u32x4 pa, pb, pc = {}, pd = {};
#define PAIR(P, i, C, o) P[i] = pk2(EXP2(C[o]), EXP2(C[(o) + 1]))
#define BC(x) __builtin_bit_cast(bf16x8, x)
#define TILE_STEP(C0, C1, N0, N1, VC0, VC1, VP0, VP1, KOFF, VOFF, HASN, HASK)          \
    {                                                                                  \
      o1 = MFMA32(VP0, BC(pc), o1);                                                    \
      PAIR(pa, 0, C0, 0); PAIR(pa, 1, C0, 2); PAIR(pa, 2, C0, 4); SB0();               \
      o1 = MFMA32(VP1, BC(pd), o1);                                                    \
      PAIR(pa, 3, C0, 6); PAIR(pb, 0, C0, 8); PAIR(pb, 1, C0, 10); SB0();              \
      if (HASN) N0 = MFMA32(kn, q0, n0);                                               \
      PAIR(pb, 2, C0, 12); PAIR(pb, 3, C0, 14); SB0();                                 \
      o0 = MFMA32(VC0, BC(pa), o0); SB0();                                             \
      if (HASN) { VP0 = lds_frag(vq + (VOFF)); VP1 = lds_frag(vq + (VOFF) + 512); }    \
      PAIR(pc, 0, C1, 0); PAIR(pc, 1, C1, 2); PAIR(pc, 2, C1, 4); SB0();               \
      if (HASN) N1 = MFMA32(kn, q1, n1);                                               \
      PAIR(pc, 3, C1, 6); PAIR(pd, 0, C1, 8); PAIR(pd, 1, C1, 10); SB0();              \
      o0 = MFMA32(VC1, BC(pb), o0); SB0();                                             \
      if (HASK) kn = lds_frag(kp + (KOFF));                                            \
      PAIR(pd, 2, C1, 12); PAIR(pd, 3, C1, 14); SB0();                                 \
    }
#pragma unroll
    for (int c = 0; c < 8; ++c) {
      TILE_STEP(sa0, sa1, sb0, sb1, vA0, vA1, vB0, vB1, (c * 4 + 2) * 1024, 1024, true, true);
      TILE_STEP(sb0, sb1, sa0, sa1, vB0, vB1, vA0, vA1, (c * 4 + 3) * 1024, 2048, true, true);
      if (c < 7) {
        if (c == 0) wait_vm<12>(); else if (c == 1) wait_vm<10>(); else if (c == 2) wait_vm<8>(); else wait_vm<0>();
        bar();
        if (c == 3 && attempt == 0) wunit_store(wun, ws, lds + (w < 6 ? K2_CMB + 8192 + w * 2048 : K2_TOP + (w - 6) * 2048), b * 8 + w, lane);
        if (FUSED && c == 6 && attempt == 0 && tid == 0) __hip_atomic_store((gu64*)(ws + WS_FL + FL_R + b * 8), tag, RLX_AGENT);
      }
      TILE_STEP(sa0, sa1, sb0, sb1, vA0, vA1, vB0, vB1, (c * 4 + 4) * 1024, 3072, true, c < 7);
      vq += vstep;
      TILE_STEP(sb0, sb1, sa0, sa1, vB0, vB1, vA0, vA1, (c * 4 + 5) * 1024, 0, c < 7, c < 7);
    }
    o1 = MFMA32(vB0, BC(pc), o1);
    o1 = MFMA32(vB1, BC(pd), o1);

    const bool ok = (o0[8] < 1e30f) && (o1[8] < 1e30f);
    if (!__all(ok)) { if (lane == 0) *(LDSP volatile unsigned*)(LDSP char*)(lds + K2_FLAG) = 1u; }
    lbar();
    const unsigned f = *(const LDSP volatile unsigned*)(LDSP char*)(lds + K2_FLAG);
    if (f == 0u || attempt == 1) break;
    nmax = 32;
  }
  {
    f32x4* cm = (f32x4*)(lds + K2_CMB + qg * 5120);
    if (kh == 1) {
      cm[0 * 64 + lane] = (f32x4){o0[0], o0[1], o0[2], o0[3]};
      cm[1 * 64 + lane] = (f32x4){o0[4], o0[5], o0[6], o0[7]};
      cm[2 * 64 + lane] = (f32x4){o1[0], o1[1], o1[2], o1[3]};
      cm[3 * 64 + lane] = (f32x4){o1[4], o1[5], o1[6], o1[7]};
      cm[4 * 64 + lane] = (f32x4){m0, o0[8], m1, o1[8]};
    }
    lbar();
    if (w == 4) poll<256>(ws, FL_R, 0, tag, lane);
    if (kh == 0) {
      const f32x4 st = cm[4 * 64 + lane];
#pragma unroll
      for (int qb = 0; qb < 2; ++qb) {
        const float ma = qb ? m1 : m0, mb = qb ? st[2] : st[0];
        const float la = qb ? o1[8] : o0[8], lb = qb ? st[3] : st[1];
        const float mm = fmaxf(ma, mb);
        const float fa = EXP2(ma - mm), fb = EXP2(mb - mm);
        const float inv = 1.f / (la * fa + lb * fb);
        const float ga = fa * inv, gb = fb * inv;
        const f32x4 b0 = cm[(qb * 2 + 0) * 64 + lane], b1 = cm[(qb * 2 + 1) * 64 + lane];
        float v[8];
#pragma unroll
        for (int i = 0; i < 4; ++i) {
          v[i] = (qb ? o1[i] : o0[i]) * ga + b0[i] * gb;
          v[4 + i] = (qb ? o1[4 + i] : o0[4 + i]) * ga + b1[i] * gb;
        }
        auto s0 = __builtin_amdgcn_permlane32_swap(pk2(v[0], v[1]), pk2(v[4], v[5]), false, false);
        auto s1 = __builtin_amdgcn_permlane32_swap(pk2(v[2], v[3]), pk2(v[6], v[7]), false, false);
        st16s(ws, (unsigned)WS_O + (unsigned)((tg * 4 + qg) * 16 + j * 2 + qb) * 1024u + (((c32 >> 4) * 2 + h) * 16 + (c32 & 15)) * 16,
              (u32x4){s0[0], s1[0], s0[1], s1[1]});
      }
    }
  }

}

template <int MODE>
DI void phaseCE(char* __restrict__ ws, char* lds, int tid, int w, const u16* __restrict__ x, u16* __restrict__ out, u64 tag) {
  constexpr int NKB = MODE ? 56 : 16, NS = NKB / 4;
  const int lane = tid & 63;
  const int b = blockIdx.x, xcd = b & 7, idx = b >> 3;
  const int tg = xcd * 4 + (idx & 3), ng = idx >> 2;
  const int r = lane & 15, g = lane >> 4;
  const int kg = w >> 2, wn = (w >> 1) & 1, wt = w & 1;
  const int nt = ng * 4 + wn * 2 + kg;

  const char* A = ws + (MODE ? WS_WD : WS_WO) + (size_t)(ng * 4) * NKB * 1024;
  const char* B = ws + (MODE ? WS_HH : WS_O) + (size_t)(tg * 4) * NKB * 1024;
  const char* src = (w < 4 ? A + (size_t)w * NKB * 1024 : B + (size_t)(w - 4) * NKB * 1024) + lane * 16;
  auto issue = [&](int s) {
    char* dst = lds + (s & 3) * 32768 + w * 1024;
#pragma unroll
    for (int i = 0; i < 4; ++i) glds16s(src + (size_t)(s * 4 + i) * 1024, dst + i * 8192);
  };
  if (w < 4) {
#pragma unroll
    for (int s = 0; s < 4; ++s) issue(s);
  }
  if (w == 4) {
    if (MODE == 0) poll<8>(ws, FL_B, tg * 8, tag, lane);
    else poll<16>(ws, FL_D, (tg >> 1) * 16, tag, lane);
  }
  bar();
  if (w >= 4) {
#pragma unroll
    for (int s = 0; s < 4; ++s) issue(s);
  }
  u32x2 res[2];
#pragma unroll
  for (int t = 0; t < 2; ++t) {
    const int tt = tg * 4 + wt * 2 + t;
    if constexpr (MODE == 0) res[t] = *(const u32x2*)(x + (size_t)(tt * 16 + r) * 512 + nt * 16 + g * 4);
    else res[t] = ld8s(ws + WS_H + (size_t)(tt * 16 + (nt >> 1)) * 1024 + (((nt & 1) * 2 + (g >> 1)) * 16 + r) * 16 + (g & 1) * 8);
  }
  asm volatile("" ::: "memory");
  f32x4 acc[2][2] = {};
  bf16x8 af[2][2][2], bf[2][2][2];
  auto rd = [&](int s, int buf) {
    const char* base = lds + (s & 3) * 32768 + lane * 16;
#pragma unroll
    for (int kk = 0; kk < 2; ++kk) {
      const char* p = base + (kg * 2 + kk) * 8192;
#pragma unroll
      for (int i = 0; i < 2; ++i) { af[buf][kk][i] = lds_frag(p + (wn * 2 + i) * 1024); bf[buf][kk][i] = lds_frag(p + (4 + wt * 2 + i) * 1024); }
    }
  };
  wait_vm<12 + 2>();
  bar();

  rd(0, 0);
#pragma unroll
  for (int s = 0; s < NS; ++s) {
    if (s + 1 < NS) {
      const int later = (NS - 2 - s) < 2 ? (NS - 2 - s) : 2;
      if (s < 2) { if (later == 2) wait_vm<8 + 2>(); else if (later == 1) wait_vm<4 + 2>(); else wait_vm<2>(); }
      else { if (later == 2) wait_vm<8>(); else if (later == 1) wait_vm<4>(); else wait_vm<0>(); }
      wait_lgkm0();
      bar();
      if (s + 4 < NS) issue(s + 4);
      rd(s + 1, (s + 1) & 1);
    }
#pragma unroll
    for (int kk = 0; kk < 2; ++kk)
#pragma unroll
      for (int i = 0; i < 2; ++i)
#pragma unroll
        for (int t = 0; t < 2; ++t) acc[i][t] = MFMA16(af[s & 1][kk][i], bf[s & 1][kk][t], acc[i][t]);
  }

  bar();
  f32x4* e4 = (f32x4*)(lds + w * 2048);
  e4[lane] = kg ? acc[0][0] : acc[1][0];
  e4[64 + lane] = kg ? acc[0][1] : acc[1][1];
  lbar();
  const f32x4* o4 = (const f32x4*)(lds + (w ^ 4) * 2048);
  if constexpr (MODE == 0) {
    float* part = (float*)(lds + 16384);
    unsigned hu[2][2];
#pragma unroll
    for (int t = 0; t < 2; ++t) {
      const f32x4 a = (t ? (kg ? acc[1][1] : acc[0][1]) : (kg ? acc[1][0] : acc[0][0])) + o4[t * 64 + lane];
      const u32x2 xr = res[t];
      const unsigned u0 = pk2(bflo(xr[0]) + a[0], bfhi(xr[0]) + a[1]), u1 = pk2(bflo(xr[1]) + a[2], bfhi(xr[1]) + a[3]);
      hu[t][0] = u0; hu[t][1] = u1;
      const float h0 = bflo(u0), h1 = bfhi(u0), h2 = bflo(u1), h3 = bfhi(u1);
      const float s1 = xsum_g((h0 + h1) + (h2 + h3)), s2 = xsum_g((h0 * h0 + h1 * h1) + (h2 * h2 + h3 * h3));
      if (g == 0) *(f32x2*)(part + ((wn * 2 + kg) * 64 + (wt * 2 + t) * 16 + r) * 2) = (f32x2){s1, s2};
    }
    {
      auto s0 = __builtin_amdgcn_permlane16_swap(hu[0][0], hu[1][0], false, false);
      auto s1 = __builtin_amdgcn_permlane16_swap(hu[0][1], hu[1][1], false, false);
      const int tt = tg * 4 + wt * 2 + (g & 1);
      st16s(ws, (unsigned)WS_H + (unsigned)(tt * 16 + (nt >> 1)) * 1024u + (((nt & 1) * 2 + (g >> 1)) * 16 + r) * 16,
            (u32x4){s0[0], s1[0], s0[1], s1[1]});
    }
    lbar();
    if (tid < 64) {
      f32x2 s = {0.f, 0.f};
#pragma unroll
      for (int i = 0; i < 4; ++i) s += *(const f32x2*)(part + (i * 64 + tid) * 2);
      st8s(ws + WS_ST + ((size_t)(tg * 64 + tid) * 8 + ng) * 8, __builtin_bit_cast(u32x2, s));
    }
  } else {
#pragma unroll
    for (int t = 0; t < 2; ++t) {
      const f32x4 a = (t ? (kg ? acc[1][1] : acc[0][1]) : (kg ? acc[1][0] : acc[0][0])) + o4[t * 64 + lane];
      const int tt = tg * 4 + wt * 2 + t;
      const u32x2 hr = res[t];
      *(u32x2*)(out + (size_t)(tt * 16 + r) * 512 + nt * 16 + g * 4) =
          (u32x2){pk2(bflo(hr[0]) + a[0], bfhi(hr[0]) + a[1]), pk2(bflo(hr[1]) + a[2], bfhi(hr[1]) + a[3])};
    }
  }

}

constexpr int K4_SLOT = 22 * 1024, K4_NSLOT = 7, K4_ST = K4_SLOT * K4_NSLOT, K4_CS = K4_ST + 1024, K4_END = K4_CS + 1024;
constexpr int LDS_BYTES = 160 * 1024;
static_assert(K2_TOP + 4096 <= 160 * 1024 && K4_END <= LDS_BYTES && K2_FLAG + 16 <= LDS_BYTES && K1_P + 8192 <= LDS_BYTES, "LDS budget");

template <int NP> DI void k4_wait(int later) {
  if (later >= 6) wait_vm<6 * NP>(); else if (later == 5) wait_vm<5 * NP>(); else if (later == 4) wait_vm<4 * NP>();
  else if (later == 3) wait_vm<3 * NP>(); else if (later == 2) wait_vm<2 * NP>(); else if (later == 1) wait_vm<NP>(); else wait_vm<0>();
}

template <int NT, int NP>
DI void k4_body(char* __restrict__ ws, char* lds, int w, int tid, int tg, int hgp, int tile0, int wt, u64 tag) {
  const int lane = tid & 63;
  const int r = lane & 15, g = lane >> 4;

  const char* A = ws + WS_WGU + (size_t)(hgp * 14) * 16 * 1024 + lane * 16;
  const char* B = ws + WS_H + (size_t)(tg * 8) * 16 * 1024 + lane * 16;
  const char* s0 = A + (size_t)w * 16384;
  const char* s1 = (w < 6) ? A + (size_t)(w + 8) * 16384 : B + (size_t)(w - 6) * 16384;
  const char* s2 = B + (size_t)(w + 2) * 16384;
#pragma unroll
  for (int s = 0; s < 7; ++s) {
    char* dst = lds + s * K4_SLOT + w * 1024;
    glds16s(s0 + s * 1024, dst);
    if (NP == 3) glds16s(s1 + s * 1024, dst + 8192);
  }
  float cp[16];
  if (NT == 4 && tid < 224) {
#pragma unroll
    for (int i = 0; i < 16; ++i) cp[i] = ld4s(ws + WS_CP + (size_t)(i * 3584 + hgp * 224 + tid) * 4);
  }
  asm volatile("" ::: "memory");
  if (w == 7) poll<16>(ws, FL_C, tg * 16, tag, lane);
  bar();
  f32x4 sp[4];
  if (NT == 4 && w < 2) {
    const char* p = ws + WS_ST + (size_t)(tg * 128 + tid) * 64;
    asm volatile("global_load_dwordx4 %0, %4, off sc1\n\tglobal_load_dwordx4 %1, %4, off offset:16 sc1\n\t"
                 "global_load_dwordx4 %2, %4, off offset:32 sc1\n\tglobal_load_dwordx4 %3, %4, off offset:48 sc1"
                 : "=&v"(sp[0]), "=&v"(sp[1]), "=&v"(sp[2]), "=&v"(sp[3]) : "v"(p) : "memory");
  }
#pragma unroll
  for (int s = 0; s < 7; ++s) {
    char* dst = lds + s * K4_SLOT + w * 1024;
    if (NP == 3) glds16s(s2 + s * 1024, dst + 16384); else glds16s(s1 + s * 1024, dst + 8192);
  }
  f32x4 acc[NT][4] = {};
  bf16x8 bf[2][4], af[2][NT];
  auto rd = [&](int s, int buf) {
    const char* base = lds + (s % K4_NSLOT) * K4_SLOT + lane * 16;
#pragma unroll
    for (int t = 0; t < 4; ++t) bf[buf][t] = lds_frag(base + (14 + wt * 4 + t) * 1024);
#pragma unroll
    for (int i = 0; i < NT; ++i) af[buf][i] = lds_frag(base + (tile0 + i) * 1024);
  };
  wait_vm<6>();
  if (NT == 4 && w < 2) {
    asm volatile("" : "+v"(sp[0]), "+v"(sp[1]), "+v"(sp[2]), "+v"(sp[3]));
    const f32x4 v = (sp[0] + sp[1]) + (sp[2] + sp[3]);
    const float mean = (v[0] + v[2]) * (1.f / 512.f);
    const float var = fmaxf((v[1] + v[3]) * (1.f / 512.f) - mean * mean, 0.f);
    *(f32x2*)(lds + K4_ST + tid * 8) = (f32x2){mean, rsqrtf(var + EPS)};
  }
  lbar();

  rd(0, 0);
#pragma unroll
  for (int s = 0; s < 16; ++s) {
    if (s + 1 < 16) {
      if (s == 0) wait_vm<5>(); else if (s == 1) wait_vm<4 + NP>(); else if (s == 2) wait_vm<3 + 2 * NP>();
      else if (s == 3) wait_vm<2 + 3 * NP>(); else if (s == 4) wait_vm<1 + 4 * NP>();
      else k4_wait<NP>((14 - s) < 5 ? (14 - s) : 5);
      wait_lgkm0();
      bar();
      rd(s + 1, (s + 1) & 1);
    }
    SB0();
#pragma unroll
    for (int i = 0; i < NT; ++i) {
#pragma unroll
      for (int t = 0; t < 4; ++t) acc[i][t] = MFMA16(af[s & 1][i], bf[s & 1][t], acc[i][t]);
      if (s + 7 < 16 && s + 1 < 16) {
        char* dst = lds + (s % K4_NSLOT) * K4_SLOT + w * 1024;
        if (i == 0) glds16s(s0 + (s + 7) * 1024, dst);
        if (i == 1) glds16s(s1 + (s + 7) * 1024, dst + 8192);
        if (i == 2 && NP == 3) glds16s(s2 + (s + 7) * 1024, dst + 16384);
      }
      SB0();
    }
  }

  if (NT == 4 && tid < 224) {
    float c = 0.f;
#pragma unroll
    for (int i = 0; i < 16; ++i) c += cp[i];
    *(float*)(lds + K4_CS + tid * 4) = c;
  }
  lbar();
  const f32x2* st = (const f32x2*)(lds + K4_ST);
  f32x4 c4[NT];
#pragma unroll
  for (int i = 0; i < NT; ++i) c4[i] = *(const f32x4*)(lds + K4_CS + ((tile0 + i) * 16 + g * 4) * 4);
  const int Tl = hgp * 14 + tile0 + g;
#pragma unroll
  for (int t = 0; t < 4; ++t) {
    const f32x2 ms = st[(wt * 4 + t) * 16 + r];
    unsigned R[4] = {0u, 0u, 0u, 0u};
#pragma unroll
    for (int i = 0; i < NT; ++i) {
      const f32x4 a = acc[i][t];
      float o[2];
#pragma unroll
      for (int e = 0; e < 2; ++e) {
        const float gv = (a[e] - ms[0] * c4[i][e]) * ms[1];
        const float uv = (a[2 + e] - ms[0] * c4[i][2 + e]) * ms[1];
        const float z = gv * (1.f + 0.044715f * gv * gv) * (-2.f * 0.7978845608028654f * 1.4426950408889634f);
        const float sg = __builtin_amdgcn_rcpf(1.f + EXP2(z));
        o[e] = gv * sg * uv;
      }
      R[i] = pk2(o[0], o[1]);
    }
    auto s02 = __builtin_amdgcn_permlane32_swap(R[0], R[2], false, false);
    auto s13 = __builtin_amdgcn_permlane32_swap(R[1], R[3], false, false);
    auto ab = __builtin_amdgcn_permlane16_swap(s02[0], s13[0], false, false);
    auto cd = __builtin_amdgcn_permlane16_swap(s02[1], s13[1], false, false);
    const int tt = tg * 8 + wt * 4 + t;
    if (g < NT)
      st16s(ws, (unsigned)WS_HH + (unsigned)(tt * 56 + (Tl >> 2)) * 1024u + ((Tl & 3) * 16 + r) * 16, (u32x4){ab[0], ab[1], cd[0], cd[1]});
  }

}

DI void phaseD(char* __restrict__ ws, char* lds, int tid, int w, u64 tag) {
  const int b = blockIdx.x, xcd = b & 7, idx = b >> 3;
  const int tg = 8 * (xcd & 1) + (idx & 7), hgp = 4 * (xcd >> 1) + (idx >> 3);
  const int wt = w & 1;
  if (w < 4) k4_body<4, 3>(ws, lds, w, tid, tg, hgp, (w >> 1) * 7, wt, tag);
  else if (w < 6) k4_body<3, 3>(ws, lds, w, tid, tg, hgp, 4, wt, tag);
  else k4_body<3, 2>(ws, lds, w, tid, tg, hgp, 11, wt, tag);
}

template <int MASK>
__global__ __launch_bounds__(512, 2) void block_fwd(const u16* __restrict__ x, const u16* __restrict__ wq,
                                                    const u16* __restrict__ wk, const u16* __restrict__ wv,
                                                    const u16* __restrict__ wo, const u16* __restrict__ wg,
                                                    const u16* __restrict__ wu, const u16* __restrict__ wd,
                                                    char* __restrict__ ws, u16* __restrict__ out) {
  extern __shared__ __attribute__((aligned(16))) char lds[];
  const int w = __builtin_amdgcn_readfirstlane(threadIdx.x >> 6);
  const int b = blockIdx.x, xcd = b & 7, idx = b >> 3;
  u64 tag = 0;
#define TID() ({ int t_ = threadIdx.x; asm volatile("" : "+v"(t_)); t_; })
  if constexpr (FUSED) tag = __hip_atomic_load((gu64*)(ws + WS_FL), RLX_AGENT) + 1ull;
  if constexpr (MASK & 3) {
    WUnit wun;
    phaseAB(x, wq, wk, wv, wo, wg, wu, wd, ws, lds, TID(), w, tag, wun);
    phaseB(ws, lds, TID(), w, tag, wun);
    publish(ws, FL_B, (xcd * 4 + (idx & 3)) * 8 + (idx >> 2), tag);
  }
  if constexpr (MASK & 4) {
    phaseCE<0>(ws, lds, TID(), w, x, nullptr, tag);
    publish(ws, FL_C, (xcd * 4 + (idx & 3)) * 8 + (idx >> 2), tag);
  }
  if constexpr (MASK & 8) {
    phaseD(ws, lds, TID(), w, tag);
    publish(ws, FL_D, (8 * (xcd & 1) + (idx & 7)) * 16 + 4 * (xcd >> 1) + (idx >> 3), tag);
  }
  if constexpr (MASK & 16) phaseCE<1>(ws, lds, TID(), w, x, out, tag);
}

template <int MASK>
static void launch(const u16* x, const u16* wq, const u16* wk, const u16* wv, const u16* wo, const u16* wg, const u16* wu,
                   const u16* wd, char* ws, u16* out, hipStream_t stream) {
  (void)hipFuncSetAttribute((const void*)block_fwd<MASK>, hipFuncAttributeMaxDynamicSharedMemorySize, LDS_BYTES);
  block_fwd<MASK><<<256, 512, LDS_BYTES, stream>>>(x, wq, wk, wv, wo, wg, wu, wd, ws, out);
}

extern "C" void kernel_launch(void* const* d_in, const int* in_sizes, int n_in, void* d_out, int out_size,
                              void* d_ws, size_t ws_size, hipStream_t stream) {
  const u16* x = (const u16*)d_in[0];
  const u16* wq = (const u16*)d_in[1];
  const u16* wk = (const u16*)d_in[2];
  const u16* wv = (const u16*)d_in[3];
  const u16* wo = (const u16*)d_in[4];
  const u16* wg = (const u16*)d_in[6];
  const u16* wu = (const u16*)d_in[7];
  const u16* wd = (const u16*)d_in[8];
  char* ws = (char*)d_ws;
  u16* out = (u16*)d_out;
  launch<31>(x, wq, wk, wv, wo, wg, wu, wd, ws, out, stream);
}
```
